```python
import torch

def _softmax(x, axis=-1):
    return torch.softmax(x, dim=axis)
def _gelu(x):
    return torch.nn.functional.gelu(x, approximate="tanh")
def _silu(x):
    return torch.nn.functional.silu(x)
def _sigmoid(x):
    return torch.sigmoid(x)
def _layernorm(x, axis=-1, eps=1e-5):
    m = x.mean(dim=axis, keepdim=True)
    v = x.var(dim=axis, keepdim=True, unbiased=False)
    return (x - m) / torch.sqrt(v + eps)
def _scatter_add(tgt, idx, src, axis=0):
    return tgt.clone().index_add_(axis, idx, src)
def _segment_sum(src, seg, n):
    out = torch.zeros((n,) + src.shape[1:], dtype=src.dtype)
    return out.index_add_(0, seg, src)
def _pad(x, pw):
    flat = tuple(v for pair in reversed(pw) for v in pair)
    return torch.nn.functional.pad(x, flat)

kernel_name = 'gated_mix__ssm_conv_h3__ssm_conv_s4__prompt'

def setup_inputs():

    _w = (lambda a: (lambda t: t.to(torch.bfloat16) if t.is_floating_point() else t)(torch.as_tensor(a)))
    def _A_setup():
        B, S, D = 1, 2048, 512
        ks = 4
        return {
            "q": _w((lambda *s: torch.randn(*s, dtype=torch.bfloat16) * 0.05)(B, S, D)),
            "k": _w((lambda *s: torch.randn(*s, dtype=torch.bfloat16) * 0.4)(B, S, D)),
            "v": _w((lambda *s: torch.randn(*s, dtype=torch.bfloat16) * 0.2)(B, S, D)),
            "shift_k": _w((lambda *s: torch.randn(*s, dtype=torch.bfloat16) * 0.4)(D, ks) * 0.5),
            "ssm_k": _w((lambda *s: torch.randn(*s, dtype=torch.bfloat16) * 0.4)(D, S) * (1.0 / S ** 0.5)),
            "ssm_D": _w((lambda *s: torch.randn(*s, dtype=torch.bfloat16) * 0.2)(D)),
        }
    _A_in = _A_setup()

    def _B_setup():
        B, L, D, N = 1, 2048, 512, 128
        A_re = -(lambda _x: torch.exp(torch.as_tensor(_x)))((lambda *s: torch.randn(*s, dtype=torch.bfloat16) * 3.2)(D, N))
        A_im = torch.pi * (lambda *s: torch.randn(*s, dtype=torch.bfloat16) * 0.05)(D, N)
        B_re = (lambda *s: torch.randn(*s, dtype=torch.bfloat16) * 0.1)(D, N)
        B_im = (lambda *s: torch.randn(*s, dtype=torch.bfloat16) * 0.1)(D, N)
        C_re = (lambda *s: torch.randn(*s, dtype=torch.bfloat16) * 0.1)(D, N)
        C_im = (lambda *s: torch.randn(*s, dtype=torch.bfloat16) * 0.1)(D, N)
        log_dt = (lambda *s: torch.randn(*s, dtype=torch.bfloat16) * 0.05)(D) * 0.5 - 4.0
        Dskip = (lambda *s: torch.randn(*s, dtype=torch.bfloat16) * 0.05)(D)
        return {
            "u": _w((lambda *s: torch.randn(*s, dtype=torch.bfloat16) * 0.05)(B, L, D)),
            "A_re": _w(A_re),
            "A_im": _w(A_im),
            "B_re": _w(B_re),
            "B_im": _w(B_im),
            "C_re": _w(C_re),
            "C_im": _w(C_im),
            "log_dt": _w(log_dt),
            "Dskip": _w(Dskip),
        }
    _B_in = _B_setup()

    return {
        "A_q": _A_in["q"],
        "A_k": _A_in["k"],
        "A_v": _A_in["v"],
        "A_shift_k": _A_in["shift_k"],
        "A_ssm_k": _A_in["ssm_k"],
        "A_ssm_D": _A_in["ssm_D"],
        "B_u": _B_in["u"],
        "B_A_re": _B_in["A_re"],
        "B_A_im": _B_in["A_im"],
        "B_B_re": _B_in["B_re"],
        "B_B_im": _B_in["B_im"],
        "B_C_re": _B_in["C_re"],
        "B_C_im": _B_in["C_im"],
        "B_log_dt": _B_in["log_dt"],
        "B_Dskip": _B_in["Dskip"],
        "gate_w": _w((lambda *s: torch.randn(*s, dtype=torch.bfloat16) * 0.05)(512) * 16.0),
    }

def reference(A_q, A_k, A_v, A_shift_k, A_ssm_k, A_ssm_D, B_u, B_A_re, B_A_im, B_B_re, B_B_im, B_C_re, B_C_im, B_log_dt, B_Dskip, gate_w):
    def _A_ref(q, k, v, shift_k, ssm_k, ssm_D):
        B, S, D = q.shape
        ks = shift_k.shape[-1]
        k_pad = _pad(k, ((0, 0), (ks - 1, 0), (0, 0)))
        k_shift = torch.zeros_like(k)
        for t in range(ks):
            k_shift = k_shift + k_pad[:, t:t + S, :] * shift_k[:, ks - 1 - t][None, None, :]
        kv = k_shift * v
        L = 2 * S
        kv_pad = _pad(kv, ((0, 0), (0, S), (0, 0)))
        kf_pad = _pad(ssm_k, ((0, 0), (0, S)))
        kv_f = (lambda x, axes=(-2,-1): torch.fft.rfft2(x.float(), dim=axes))(kv_pad[:, None, :, :], axes=(1, 2))
        kf_f = (lambda x, axes=(-2,-1): torch.fft.rfft2(x.float(), dim=axes))(kf_pad[:, None, :], axes=(1, 2))
        kf_f = (lambda x, *d: x.permute(*d))(kf_f, 1, 2, 0)
        y_f = kv_f * kf_f[None, :, :, :]
        y = (lambda x, s, axes=(-2,-1): torch.fft.irfft2(x, s=s, dim=axes).to(torch.bfloat16))(y_f, (1, L), axes=(1, 2))[:, 0, :S, :]
        y = y + kv * ssm_D[None, None, :]
        return q * y

    def _B_ref(u, A_re, A_im, B_re, B_im, C_re, C_im, log_dt, Dskip):
        Bsz, L, D = u.shape
        dt = torch.exp(log_dt)[:, None]
        A = A_re + 1j * A_im
        Bc = B_re + 1j * B_im
        Cc = C_re + 1j * C_im
        logdA = dt * A
        dB = dt * Bc
        l = (lambda x, dt: x.to(dt))(torch.arange(L), A_re.dtype)
        P = torch.exp(logdA[:, :, None] * l[None, None, :])
        K = torch.einsum("dn,dnl,dn->dl", Cc, P, dB).real
        L2 = 2 * L
        Kp = _pad(K, ((0, 0), (0, L)))
        up = (lambda x, *d: x.permute(*d))(u, 0, 2, 1)
        up = _pad(up, ((0, 0), (0, 0), (0, L)))
        Kf = (lambda x, axes=(-2,-1): torch.fft.rfft2(x.float(), dim=axes))(Kp[:, None, :], axes=(-2, -1))
        Uf = (lambda x, axes=(-2,-1): torch.fft.rfft2(x.float(), dim=axes))(up[:, :, None, :], axes=(-2, -1))
        Yf = Uf * Kf[None, :, :, :]
        y = (lambda x, s, axes=(-2,-1): torch.fft.irfft2(x, s=s, dim=axes).to(torch.bfloat16))(Yf, (1, L2), axes=(-2, -1))[:, :, 0, :L]
        y = (lambda x, *d: x.permute(*d))(y, 0, 2, 1) + u * Dskip[None, None, :]
        return y

    x = A_q
    g = _sigmoid((x * gate_w).sum(-1, keepdims=True))
    a = _A_ref(x, A_k, A_v, A_shift_k, A_ssm_k, A_ssm_D)
    b = _B_ref(x, B_A_re, B_A_im, B_B_re, B_B_im, B_C_re, B_C_im, B_log_dt, B_Dskip)
    return g * a + (1 - g) * b
```

```cpp
#include <hip/hip_runtime.h>
#include <hip/hip_bf16.h>
#include <stdint.h>

typedef unsigned short u16;
#define S_ 2048
#define D_ 512
#define N_ 128

__device__ __forceinline__ float bf2f(u16 v) { return __uint_as_float(((unsigned)v) << 16); }
__device__ __forceinline__ u16 f2bf(float f) {
    unsigned u = __float_as_uint(f);
    u += 0x7FFF + ((u >> 16) & 1);
    return (u16)(u >> 16);
}
__device__ __forceinline__ float rbf(float f) { return bf2f(f2bf(f)); }

__global__ void k_gate(const u16* x, const u16* w, float* g) {
    int t = blockIdx.x;
    int lane = threadIdx.x;
    float s = 0.f;
    for (int d = lane; d < D_; d += 64) s += rbf(bf2f(x[t * D_ + d]) * bf2f(w[d]));
    for (int o = 32; o > 0; o >>= 1) s += __shfl_xor(s, o);
    if (lane == 0) {
        float z = rbf(s);
        g[t] = rbf(1.f / (1.f + expf(-z)));
    }
}

__global__ void k_kv(const u16* k, const u16* v, const u16* shift_k, u16* kv) {
    int idx = blockIdx.x * blockDim.x + threadIdx.x;
    int t = idx / D_, d = idx % D_;
    float acc = 0.f;
    for (int j = 0; j < 4; j++) {
        int tt = t - 3 + j;
        float kk = tt >= 0 ? bf2f(k[tt * D_ + d]) : 0.f;
        float p = rbf(kk * bf2f(shift_k[d * 4 + 3 - j]));
        acc = rbf(acc + p);
    }
    kv[idx] = f2bf(rbf(acc * bf2f(v[idx])));
}

__global__ void k_kgen(const u16* Are, const u16* Aim, const u16* Bre, const u16* Bim, const u16* Cre, const u16* Cim,
                       const u16* logdt, float* K) {
    int idx = blockIdx.x * blockDim.x + threadIdx.x;
    int d = idx / S_, l = idx % S_;
    float lf = rbf((float)l);
    float dt = rbf(expf(bf2f(logdt[d])));
    float acc = 0.f;
    for (int n = 0; n < N_; n++) {
        float ar = dt * bf2f(Are[d * N_ + n]), ai = dt * bf2f(Aim[d * N_ + n]);
        float br = dt * bf2f(Bre[d * N_ + n]), bi = dt * bf2f(Bim[d * N_ + n]);
        float cr = bf2f(Cre[d * N_ + n]), ci = bf2f(Cim[d * N_ + n]);
        float e = expf(ar * lf);
        float sn, cs;
        sincosf(ai * lf, &sn, &cs);
        float pr = e * cs, pi = e * sn;
        float cbr = cr * br - ci * bi, cbi = cr * bi + ci * br;
        acc += cbr * pr - cbi * pi;
    }
    K[idx] = acc;
}

__global__ void k_conv(const u16* x, const u16* kv, const u16* ssm_k, const float* K, const u16* ssmD, const u16* Dskip,
                       const float* g, u16* out) {
    int idx = blockIdx.x * blockDim.x + threadIdx.x;
    int t = idx / D_, d = idx % D_;
    float ya = 0.f, yb = 0.f;
    for (int l = 0; l <= t; l++) {
        ya += bf2f(ssm_k[d * S_ + l]) * bf2f(kv[(t - l) * D_ + d]);
        yb += K[d * S_ + l] * bf2f(x[(t - l) * D_ + d]);
    }
    float xv = bf2f(x[idx]), kvv = bf2f(kv[idx]);
    float a = rbf(rbf(ya) + rbf(kvv * bf2f(ssmD[d])));
    a = rbf(xv * a);
    float b = rbf(rbf(yb) + rbf(xv * bf2f(Dskip[d])));
    float gg = g[t];
    float o = rbf(rbf(gg * a) + rbf(rbf(1.f - gg) * b));
    out[idx] = f2bf(o);
}

extern "C" void kernel_launch(void* const* d_in, const int* in_sizes, int n_in,
                              void* d_out, int out_size, void* d_ws, size_t ws_size,
                              hipStream_t stream) {
    const u16* q = (const u16*)d_in[0];
    const u16* k = (const u16*)d_in[1];
    const u16* v = (const u16*)d_in[2];
    const u16* shift_k = (const u16*)d_in[3];
    const u16* ssm_k = (const u16*)d_in[4];
    const u16* ssm_D = (const u16*)d_in[5];
    const u16* Are = (const u16*)d_in[7];
    const u16* Aim = (const u16*)d_in[8];
    const u16* Bre = (const u16*)d_in[9];
    const u16* Bim = (const u16*)d_in[10];
    const u16* Cre = (const u16*)d_in[11];
    const u16* Cim = (const u16*)d_in[12];
    const u16* logdt = (const u16*)d_in[13];
    const u16* Dskip = (const u16*)d_in[14];
    const u16* gw = (const u16*)d_in[15];
    char* ws = (char*)d_ws;
    float* g = (float*)ws;
    u16* kv = (u16*)(ws + 65536);
    float* K = (float*)(ws + 65536 + 4 * 1024 * 1024);
    k_gate<<<S_, 64, 0, stream>>>(q, gw, g);
    k_kv<<<S_ * D_ / 256, 256, 0, stream>>>(k, v, shift_k, kv);
    k_kgen<<<S_ * D_ / 256, 256, 0, stream>>>(Are, Aim, Bre, Bim, Cre, Cim, logdt, K);
    k_conv<<<S_ * D_ / 256, 256, 0, stream>>>(q, kv, ssm_k, K, ssm_D, Dskip, g, (u16*)d_out);
}
```

```cpp
#include <hip/hip_runtime.h>
#include <stdint.h>

typedef unsigned short u16;
typedef unsigned int u32;
typedef unsigned long long u64;
typedef short v4s __attribute__((ext_vector_type(4)));
typedef short v8s __attribute__((ext_vector_type(8)));
typedef __bf16 v8bf __attribute__((ext_vector_type(8)));
typedef __bf16 v2bf __attribute__((ext_vector_type(2)));
typedef float v4f __attribute__((ext_vector_type(4)));
typedef float v2f __attribute__((ext_vector_type(2)));
typedef u32 v4u __attribute__((ext_vector_type(4)));
typedef u32 v2u __attribute__((ext_vector_type(2)));
#define LAS __attribute__((address_space(3)))
#define GAS __attribute__((address_space(1)))

#define NPASS 3
#define S_ 2048
#define D_ 512
#define N_ 128

#define STAMP(i) do { } while (0)

__device__ __forceinline__ float bflo(u32 v) { return __uint_as_float(v << 16); }
__device__ __forceinline__ float bfhi(u32 v) { return __uint_as_float(v & 0xffff0000u); }
__device__ __forceinline__ float bf1(u16 v) { return __uint_as_float(((u32)v) << 16); }
__device__ __forceinline__ u32 pk(float lo, float hi) {
    v2f x = {lo, hi};
    return __builtin_bit_cast(u32, __builtin_convertvector(x, v2bf));
}
__device__ __forceinline__ float rbf(float f) { return bflo(pk(f, f)); }

struct cpx { float r, i; };
__device__ __forceinline__ cpx cmul(cpx a, cpx b) {
    cpx c;
    c.r = a.r * b.r - a.i * b.i;
    c.i = a.r * b.i + a.i * b.r;
    return c;
}

__device__ __forceinline__ cpx epow(float a2, float rh, float rl, float s) {
    float e = __builtin_amdgcn_exp2f(a2 * s);
    float x = __builtin_amdgcn_fractf(rh * s) + rl * s;
    cpx c;
    c.r = e * __builtin_amdgcn_cosf(x);
    c.i = e * __builtin_amdgcn_sinf(x);
    return c;
}

#define P1_PITCH 544
#define P1_TAB (64 * P1_PITCH)
#define P1_T_OFF (2 * P1_TAB)
#define P1_LDS (P1_T_OFF + 2 * 4112)

__global__ void __launch_bounds__(512, 2)
prep_kernel(const u16* __restrict__ X, const u16* __restrict__ Kin, const u16* __restrict__ V, const u16* __restrict__ shiftk,
            const u16* __restrict__ gatew, const u16* __restrict__ Are, const u16* __restrict__ Aim, const u16* __restrict__ Bre,
            const u16* __restrict__ Bim, const u16* __restrict__ Cre, const u16* __restrict__ Cim, const u16* __restrict__ logdt,
            u16* __restrict__ uT, u16* __restrict__ hB, u32* __restrict__ gate
            ) {
    __shared__ __attribute__((aligned(16))) char smem[P1_LDS];
    const int tid = threadIdx.x;
    const int bid = blockIdx.x;
    const int lane = tid & 63;
    STAMP(0);
    if (bid < 256) {
        const int dg = bid & 7, tg = bid >> 3;
        const int cp = tid & 31, rb = tid >> 5;
        const int t0 = tg * 64 + rb * 4;
        const int c = dg * 64 + cp * 2;
        const u32* X32 = (const u32*)X;
        const u32* K32 = (const u32*)Kin;
        const u32* V32 = (const u32*)V;
        u32 kk[7], vv[4], xx[4];
#pragma unroll
        for (int j = 0; j < 7; j++) {
            int t = t0 - 3 + j;
            kk[j] = (t >= 0) ? K32[(t * D_ + c) >> 1] : 0u;
        }
#pragma unroll
        for (int j = 0; j < 4; j++) {
            vv[j] = V32[((t0 + j) * D_ + c) >> 1];
            xx[j] = X32[((t0 + j) * D_ + c) >> 1];
        }
        const v4u sk = *(const v4u*)(shiftk + c * 4);
        const int w = tid >> 6;
        const int tgate = bid * 8 + w;
        const v4u gx = *(const v4u*)(X + tgate * D_ + lane * 8);
        const v4u gw = *(const v4u*)(gatew + lane * 8);
        float s0[4] = {bflo(sk[0]), bfhi(sk[0]), bflo(sk[1]), bfhi(sk[1])};
        float s1[4] = {bflo(sk[2]), bfhi(sk[2]), bflo(sk[3]), bfhi(sk[3])};
        u32 kvp[4];
#pragma unroll
        for (int j = 0; j < 4; j++) {
            float a0 = bflo(kk[j]) * s0[3] + bflo(kk[j + 1]) * s0[2] + bflo(kk[j + 2]) * s0[1] + bflo(kk[j + 3]) * s0[0];
            float a1 = bfhi(kk[j]) * s1[3] + bfhi(kk[j + 1]) * s1[2] + bfhi(kk[j + 2]) * s1[1] + bfhi(kk[j + 3]) * s1[0];
            kvp[j] = pk(a0 * bflo(vv[j]), a1 * bfhi(vv[j]));
        }
        STAMP(1);
        {
            int sw = (rb ^ ((cp & 7) << 1)) * 8;
            char* base = smem + (cp * 2) * 128 + sw;
            v2u a, b;
            a[0] = __builtin_amdgcn_perm(kvp[1], kvp[0], 0x05040100); a[1] = __builtin_amdgcn_perm(kvp[3], kvp[2], 0x05040100);
            b[0] = __builtin_amdgcn_perm(kvp[1], kvp[0], 0x07060302); b[1] = __builtin_amdgcn_perm(kvp[3], kvp[2], 0x07060302);
            *(v2u*)(base) = a; *(v2u*)(base + 128) = b;
            a[0] = __builtin_amdgcn_perm(xx[1], xx[0], 0x05040100); a[1] = __builtin_amdgcn_perm(xx[3], xx[2], 0x05040100);
            b[0] = __builtin_amdgcn_perm(xx[1], xx[0], 0x07060302); b[1] = __builtin_amdgcn_perm(xx[3], xx[2], 0x07060302);
            *(v2u*)(base + 8192) = a; *(v2u*)(base + 8192 + 128) = b;
        }
        {
            float s = 0.f;
#pragma unroll
            for (int j = 0; j < 4; j++) {
                u32 p = pk(bflo(gx[j]) * bflo(gw[j]), bfhi(gx[j]) * bfhi(gw[j]));
                s += bflo(p) + bfhi(p);
            }
#pragma unroll
            for (int o = 32; o > 0; o >>= 1) s += __shfl_xor(s, o);
            if (lane == 0) {
                float z = rbf(s);
                float g = rbf(1.f / (1.f + __expf(-z)));
                gate[tgate] = pk(g, 1.f - g);
            }
        }
        __syncthreads();
#pragma unroll
        for (int i = 0; i < 2; i++) {
            int idx = tid + 512 * i;
            int ten = idx >> 9, ch = (idx >> 3) & 63, ck = idx & 7;
            int sw = (ck ^ ((ch >> 1) & 7)) * 16;
            v4u d = *(const v4u*)(smem + ten * 8192 + ch * 128 + sw);
            *(v4u*)(uT + ((size_t)ten * D_ + dg * 64 + ch) * S_ + tg * 64 + ck * 8) = d;
        }
        STAMP(2);
    } else {
        const int b2 = bid - 256;
        const int pr = (b2 & 7) * 32 + (b2 >> 3);
        const int ch = (tid >> 7) & 1, n = tid & 127, half = __builtin_amdgcn_readfirstlane(tid >> 8);
        const int d = pr * 2 + ch;
        const int pi = d * N_ + n;
        float are = bf1(Are[pi]), aim = bf1(Aim[pi]), bre = bf1(Bre[pi]), bim = bf1(Bim[pi]), cre = bf1(Cre[pi]), cim = bf1(Cim[pi]);
        float dt = rbf(expf(bf1(logdt[d])));
        float a = dt * are, b = dt * aim;
        float br = dt * bre, bi = dt * bim;
        cpx CB; CB.r = cre * br - cim * bi; CB.i = cre * bi + cim * br;
        const float a2 = a * 1.4426950408889634f;
        const float rh = b * 0.15915494309189535f;
        const float rl = __builtin_fmaf(b, 0.15915494309189535f, -rh) + b * 6.4206383e-9f;
        STAMP(1);
        char* tab = smem + ch * P1_TAB + n * 4;
#define PUTG(row, v) (*(u32*)(tab + (row) * P1_PITCH) = pk((v).r, -(v).i))
#define PUTF(row, v) (*(u32*)(tab + (48 + (row)) * P1_PITCH) = pk((v).r, (v).i))
        if (half == 0) {
            cpx E0 = epow(a2, rh, rl, 1.f), E1 = epow(a2, rh, rl, 2.f), E2 = epow(a2, rh, rl, 4.f);
            cpx E7 = epow(a2, rh, rl, 128.f), E8 = epow(a2, rh, rl, 256.f), E11 = epow(a2, rh, rl, 2048.f);
            cpx P[8];
            P[0] = CB; P[1] = cmul(P[0], E0); P[2] = cmul(P[0], E1); P[3] = cmul(P[1], E1);
            P[4] = cmul(P[0], E2); P[5] = cmul(P[1], E2); P[6] = cmul(P[2], E2); P[7] = cmul(P[3], E2);
#pragma unroll
            for (int e = 0; e < 8; e++) { PUTG(e, P[e]); cpx t = cmul(P[e], E7); PUTG(8 + e, t); }
#pragma unroll
            for (int e = 0; e < 4; e++) { cpx t = cmul(P[2 * e], E8); PUTG(16 + e, t); cpx t2 = cmul(t, E7); PUTG(20 + e, t2); }
            cpx t = cmul(CB, E11); PUTG(40, t);
        } else {
            cpx E2 = epow(a2, rh, rl, 4.f), E7 = epow(a2, rh, rl, 128.f), E8 = epow(a2, rh, rl, 256.f);
            cpx E9 = epow(a2, rh, rl, 512.f), E10 = epow(a2, rh, rl, 1024.f);
            cpx W3 = cmul(E7, E8);
            cpx Q0 = cmul(CB, E9), Q1 = cmul(Q0, E2);
            PUTG(24, Q0); PUTG(25, Q1);
            { cpx t = cmul(Q0, E7); PUTG(26, t); t = cmul(Q1, E7); PUTG(27, t); }
            { cpx t = cmul(Q0, E8); PUTG(28, t); t = cmul(Q1, E8); PUTG(29, t); }
            { cpx t = cmul(Q0, W3); PUTG(30, t); t = cmul(Q1, W3); PUTG(31, t); }
            cpx T0 = cmul(CB, E10), T1 = cmul(T0, E7), T2 = cmul(T0, E8), T3 = cmul(T0, W3);
            PUTG(32, T0); PUTG(33, T1); PUTG(34, T2); PUTG(35, T3);
            { cpx t = cmul(T0, E9); PUTG(36, t); t = cmul(T1, E9); PUTG(37, t); t = cmul(T2, E9); PUTG(38, t); t = cmul(T3, E9); PUTG(39, t); }
            cpx F[8];
            F[0].r = 1.f; F[0].i = 0.f;
            F[1] = epow(a2, rh, rl, 8.f); F[2] = epow(a2, rh, rl, 16.f); F[4] = epow(a2, rh, rl, 32.f);
            cpx F8 = epow(a2, rh, rl, 64.f);
            F[3] = cmul(F[1], F[2]); F[5] = cmul(F[4], F[1]); F[6] = cmul(F[4], F[2]); F[7] = cmul(F[4], F[3]);
            PUTF(0, F[0]); PUTF(8, F8);
#pragma unroll
            for (int e = 1; e < 8; e++) { PUTF(e, F[e]); cpx t = cmul(F8, F[e]); PUTF(8 + e, t); }
        }
        __syncthreads();
        STAMP(2);
        const int w = tid >> 6;
        if (w < 6) {
            const int c2 = w / 3, mt = w - c2 * 3;
            const char* ga = smem + c2 * P1_TAB + (mt * 16 + (lane & 15)) * P1_PITCH + (lane >> 4) * 16;
            const char* fb = smem + c2 * P1_TAB + (48 + (lane & 15)) * P1_PITCH + (lane >> 4) * 16;
            v4f acc = {0.f, 0.f, 0.f, 0.f};
#pragma unroll
            for (int s = 0; s < 8; s++) {
                v8bf A = *(const v8bf*)(ga + s * 64);
                v8bf B = *(const v8bf*)(fb + s * 64);
                acc = __builtin_amdgcn_mfma_f32_16x16x32_bf16(A, B, acc, 0, 0, 0);
            }
            u16* T = (u16*)(smem + P1_T_OFF + c2 * 4112);
            const int bcol = lane & 15;
#pragma unroll
            for (int r = 0; r < 4; r++) {
                int m = (lane >> 4) * 4 + r;
                int g;
                if (mt == 0) g = (m & 7) + 128 * (m >> 3);
                else if (mt == 1) g = (m < 8) ? (256 + 128 * (m >> 2) + 2 * (m & 3)) : (512 + 128 * ((m - 8) >> 1) + 4 * (m & 1));
                else g = (m < 8) ? (1024 + 128 * m) : 2048;
                bool ok = (mt < 2) || (m < 8) || (m == 8 && bcol == 0);
                if (ok) T[g + 8 * bcol] = (u16)(pk(acc[r], 0.f) & 0xffff);
            }
        }
        __syncthreads();
        {
            const int c2 = tid >> 8, l0 = (tid & 255) * 8;
            const u16* T = (const u16*)(smem + P1_T_OFF + c2 * 4112);
            int sh = (l0 < 256) ? 0 : (24 - __builtin_clz(l0));
            u32 o[4];
#pragma unroll
            for (int j = 0; j < 8; j += 2) {
                int la = l0 + j, lb = l0 + j + 1;
                int half_ = (1 << sh) >> 1;
                int va = sh ? (((la + half_ - 1 + ((la >> sh) & 1)) >> sh) << sh) : la;
                int vb = sh ? (((lb + half_ - 1 + ((lb >> sh) & 1)) >> sh) << sh) : lb;
                o[j >> 1] = (u32)T[va] | ((u32)T[vb] << 16);
            }
            v4u ov = {o[0], o[1], o[2], o[3]};
            *(v4u*)(hB + (size_t)(pr * 2 + c2) * S_ + l0) = ov;
        }
        STAMP(3);
    }
}

#define H_COPY 4160
#define H_UNIT (4 * H_COPY)
#define U_OFF (4 * H_UNIT)
#define U_UNIT 4608
#define Y_OFF (U_OFF + 4 * U_UNIT)
#define G_OFF (Y_OFF + 4 * 4096)
#define C_LDS (G_OFF + 8192)

__device__ __forceinline__ void dma16(const void* g, char* l) {
    __builtin_amdgcn_global_load_lds((const GAS void*)g, (LAS void*)l, 16, 0, 0);
}

template <int TLO, int THI>
__device__ __forceinline__ void conv_pass(const char* ha, const char* ub, char* yb) {
    constexpr int NT = THI - TLO + 1;
    v4f acc[NT];
#pragma unroll
    for (int i = 0; i < NT; i++) acc[i] = (v4f){0.f, 0.f, 0.f, 0.f};
#pragma unroll 1
    for (int r = 0; r < 8; r++) {
        v8bf A[THI + 1], B[THI + 1];
#pragma unroll
        for (int i = 0; i <= THI; i++) {
            v4s lo = __builtin_amdgcn_ds_read_tr16_b64_v4i16((LAS v4s*)(ha + r * 64 + i * 512 + 8));
            v4s hi = __builtin_amdgcn_ds_read_tr16_b64_v4i16((LAS v4s*)(ha + r * 64 + i * 512));
            v8s ab = __builtin_shufflevector(lo, hi, 0, 1, 2, 3, 4, 5, 6, 7);
            A[i] = __builtin_bit_cast(v8bf, ab);
            B[i] = *(const LAS v8bf*)(LAS char*)(ub - r * 64 + i * 512);
        }
#pragma unroll
        for (int i = 0; i <= THI; i++)
#pragma unroll
            for (int q = 0; q <= THI; q++)
                if (i + q >= TLO && i + q <= THI)
                    acc[i + q - TLO] = __builtin_amdgcn_mfma_f32_16x16x32_bf16(A[i], B[q], acc[i + q - TLO], 0, 0, 0);
    }
#pragma unroll
    for (int i = 0; i < NT; i++) {
        v2u o = {pk(acc[i][0], acc[i][1]), pk(acc[i][2], acc[i][3])};
        *(v2u*)(yb + (TLO + i) * 512) = o;
    }
}

__device__ __forceinline__ void epi_rows(const char* smem, int t, u32* __restrict__ out32, int p, float dA0, float dA1, float dS0, float dS1) {
    const u32 ya0 = *(const u32*)(smem + Y_OFF + 0 * 4096 + t * 2), yb0 = *(const u32*)(smem + Y_OFF + 1 * 4096 + t * 2);
    const u32 ya1 = *(const u32*)(smem + Y_OFF + 2 * 4096 + t * 2), yb1 = *(const u32*)(smem + Y_OFF + 3 * 4096 + t * 2);
    const u32 kv0 = *(const u32*)(smem + U_OFF + 0 * U_UNIT + 512 + t * 2), x0 = *(const u32*)(smem + U_OFF + 1 * U_UNIT + 512 + t * 2);
    const u32 kv1 = *(const u32*)(smem + U_OFF + 2 * U_UNIT + 512 + t * 2), x1 = *(const u32*)(smem + U_OFF + 3 * U_UNIT + 512 + t * 2);
    const v2u gg = *(const v2u*)(smem + G_OFF + t * 4);
    {
        float g = bflo(gg[0]), g1 = bfhi(gg[0]);
        float a0 = bflo(x0) * (bflo(ya0) + bflo(kv0) * dA0), b0 = bflo(yb0) + bflo(x0) * dS0;
        float a1 = bflo(x1) * (bflo(ya1) + bflo(kv1) * dA1), b1 = bflo(yb1) + bflo(x1) * dS1;
        out32[t * 256 + p] = pk(g * a0 + g1 * b0, g * a1 + g1 * b1);
    }
    {
        float g = bflo(gg[1]), g1 = bfhi(gg[1]);
        float a0 = bfhi(x0) * (bfhi(ya0) + bfhi(kv0) * dA0), b0 = bfhi(yb0) + bfhi(x0) * dS0;
        float a1 = bfhi(x1) * (bfhi(ya1) + bfhi(kv1) * dA1), b1 = bfhi(yb1) + bfhi(x1) * dS1;
        out32[(t + 1) * 256 + p] = pk(g * a0 + g1 * b0, g * a1 + g1 * b1);
    }
}

#define RAW_BARRIER() do { asm volatile("s_waitcnt lgkmcnt(0)" ::: "memory"); __builtin_amdgcn_s_barrier(); asm volatile("" ::: "memory"); } while (0)

__global__ void __launch_bounds__(512, 2)
conv_kernel(const u16* __restrict__ uT, const u16* __restrict__ hA, const u16* __restrict__ hB, const u32* __restrict__ gate,
            const u16* __restrict__ ssmD, const u16* __restrict__ Dskip, u32* __restrict__ out32
            ) {
    __shared__ __attribute__((aligned(16))) char smem[C_LDS];
    const int tid = threadIdx.x;
    const int bid = blockIdx.x;
    const int lane = tid & 63;
    const int w = __builtin_amdgcn_readfirstlane(tid >> 6);
    const int p = (bid & 7) * 32 + (bid >> 3);
    STAMP(0);
    {
        const int u = w >> 1;
        const int c = p * 2 + (u >> 1);
        const u16* hrow = ((u & 1) ? hB : hA) + (size_t)c * S_;
        const u16* urow = uT + ((size_t)(u & 1) * D_ + c) * S_;
#pragma unroll
        for (int qq = 0; qq < 2; qq++) {
            const int q = (w & 1) * 2 + qq;
#pragma unroll
            for (int i = 0; i < 4; i++) {
                int e = 8 + i * 512 + lane * 8;
                int src = e - q;
                if (e >= S_) src = 0;
                dma16(hrow + src, smem + u * H_UNIT + q * H_COPY + 32 + 16 + i * 1024);
            }
        }
#pragma unroll
        for (int i = 0; i < 2; i++) {
            int e = ((w & 1) * 2 + i) * 512 + lane * 8;
            dma16(urow + e, smem + U_OFF + u * U_UNIT + 512 + ((w & 1) * 2 + i) * 1024);
        }
        dma16(gate + w * 256 + lane * 4, smem + G_OFF + w * 1024);
    }
    if (w == 7) {
        if (lane < 16) {
            const int u = lane >> 2, q = lane & 3;
            const int c = p * 2 + (u >> 1);
            const u16* hrow = ((u & 1) ? hB : hA) + (size_t)c * S_;
            v4u h = *(const v4u*)hrow;
            v4u o;
            if (q == 0) o = h;
            else if (q == 1) { o[0] = h[0] << 16; o[1] = __builtin_amdgcn_alignbit(h[1], h[0], 16); o[2] = __builtin_amdgcn_alignbit(h[2], h[1], 16); o[3] = __builtin_amdgcn_alignbit(h[3], h[2], 16); }
            else if (q == 2) { o[0] = 0; o[1] = h[0]; o[2] = h[1]; o[3] = h[2]; }
            else { o[0] = 0; o[1] = h[0] << 16; o[2] = __builtin_amdgcn_alignbit(h[1], h[0], 16); o[3] = __builtin_amdgcn_alignbit(h[2], h[1], 16); }
            char* dst = smem + u * H_UNIT + q * H_COPY;
            v4u z = {0, 0, 0, 0};
            *(v4u*)(dst) = z; *(v4u*)(dst + 16) = z; *(v4u*)(dst + 32) = o;
        }
    } else if (w == 6) {
        v4u z = {0, 0, 0, 0};
        *(v4u*)(smem + U_OFF + (lane >> 4) * U_UNIT + (lane & 15) * 16) = z;
        *(v4u*)(smem + U_OFF + (lane >> 4) * U_UNIT + 256 + (lane & 15) * 16) = z;
    }
    float dA0 = bf1(ssmD[p * 2]), dA1 = bf1(ssmD[p * 2 + 1]), dS0 = bf1(Dskip[p * 2]), dS1 = bf1(Dskip[p * 2 + 1]);
    asm volatile("s_waitcnt vmcnt(0)" ::: "memory");
    RAW_BARRIER();
    STAMP(1);
    if (w < 4) {
        const int g4 = lane >> 4, n = lane & 15, q = (lane >> 2) & 3, pp = lane & 3;
        const char* ha = smem + w * H_UNIT + q * H_COPY + 32 + 32 - 16 * g4 + 8 * pp - 8;
        const char* ub = smem + U_OFF + w * U_UNIT + 512 + 32 * n + 16 * g4 - 32;
        char* yb = smem + Y_OFF + w * 4096 + 32 * n + 8 * g4;
        conv_pass<0, 3>(ha, ub, yb);
        RAW_BARRIER();
        STAMP(2);
        conv_pass<4, 5>(ha, ub, yb);
        RAW_BARRIER();
        STAMP(3);
        conv_pass<6, 7>(ha, ub, yb);
        RAW_BARRIER();
        STAMP(4);
    } else {
        const int e = tid - 256;
        RAW_BARRIER();
        STAMP(2);
        epi_rows(smem, e * 2, out32, p, dA0, dA1, dS0, dS1);
        epi_rows(smem, 512 + e * 2, out32, p, dA0, dA1, dS0, dS1);
        RAW_BARRIER();
        STAMP(3);
        epi_rows(smem, 1024 + e * 2, out32, p, dA0, dA1, dS0, dS1);
        RAW_BARRIER();
        STAMP(4);
        epi_rows(smem, 1536 + e * 2, out32, p, dA0, dA1, dS0, dS1);
        STAMP(5);
    }
}

extern "C" void kernel_launch(void* const* d_in, const int* in_sizes, int n_in,
                              void* d_out, int out_size, void* d_ws, size_t ws_size,
                              hipStream_t stream) {
    const u16* q = (const u16*)d_in[0];
    const u16* k = (const u16*)d_in[1];
    const u16* v = (const u16*)d_in[2];
    const u16* shift_k = (const u16*)d_in[3];
    const u16* ssm_k = (const u16*)d_in[4];
    const u16* ssm_D = (const u16*)d_in[5];
    const u16* Are = (const u16*)d_in[7];
    const u16* Aim = (const u16*)d_in[8];
    const u16* Bre = (const u16*)d_in[9];
    const u16* Bim = (const u16*)d_in[10];
    const u16* Cre = (const u16*)d_in[11];
    const u16* Cim = (const u16*)d_in[12];
    const u16* logdt = (const u16*)d_in[13];
    const u16* Dskip = (const u16*)d_in[14];
    const u16* gw = (const u16*)d_in[15];
    char* ws = (char*)d_ws;
    u16* uT = (u16*)ws;
    u16* hB = (u16*)(ws + 4 * 1024 * 1024);
    u32* gate = (u32*)(ws + 6 * 1024 * 1024);
    prep_kernel<<<512, 512, 0, stream>>>(q, k, v, shift_k, gw, Are, Aim, Bre, Bim, Cre, Cim, logdt, uT, hB, gate);
    conv_kernel<<<256, 512, 0, stream>>>(uT, ssm_k, hB, gate, ssm_D, Dskip, (u32*)d_out);
}
```

```cpp
#include <hip/hip_runtime.h>
#include <stdint.h>

typedef unsigned short u16;
typedef unsigned int u32;
typedef unsigned long long u64;
typedef short v4s __attribute__((ext_vector_type(4)));
typedef short v8s __attribute__((ext_vector_type(8)));
typedef __bf16 v8bf __attribute__((ext_vector_type(8)));
typedef __bf16 v2bf __attribute__((ext_vector_type(2)));
typedef float v4f __attribute__((ext_vector_type(4)));
typedef float v2f __attribute__((ext_vector_type(2)));
typedef u32 v4u __attribute__((ext_vector_type(4)));
typedef u32 v2u __attribute__((ext_vector_type(2)));
#define LAS __attribute__((address_space(3)))
#define GAS __attribute__((address_space(1)))
typedef LAS char* lptr;

#define S_ 2048
#define D_ 512
#define N_ 128

#define STAMP(i) do { } while (0)

__device__ __forceinline__ float bflo(u32 v) { return __uint_as_float(v << 16); }
__device__ __forceinline__ float bfhi(u32 v) { return __uint_as_float(v & 0xffff0000u); }
__device__ __forceinline__ float bf1(u16 v) { return __uint_as_float(((u32)v) << 16); }
__device__ __forceinline__ u32 pk(float lo, float hi) {
    v2f x = {lo, hi};
    return __builtin_bit_cast(u32, __builtin_convertvector(x, v2bf));
}
__device__ __forceinline__ u32 pk2(v2f x) { return __builtin_bit_cast(u32, __builtin_convertvector(x, v2bf)); }
__device__ __forceinline__ float rbf(float f) { return bflo(pk(f, f)); }

template <typename T>
__device__ __forceinline__ T ldg(const void* base, u32 off) { return *(const GAS T*)((const GAS char*)base + off); }

__device__ __forceinline__ void cmul2(v2f& c0, v2f a0, v2f b0, v2f& c1, v2f a1, v2f b1) {
    asm("v_pk_mul_f32 %0, %2, %3 op_sel:[0,0] op_sel_hi:[1,0]\n\t"
        "v_pk_mul_f32 %1, %4, %5 op_sel:[0,0] op_sel_hi:[1,0]\n\t"
        "v_pk_fma_f32 %0, %2, %3, %0 op_sel:[1,1,0] op_sel_hi:[0,1,1] neg_lo:[1,0,0]\n\t"
        "v_pk_fma_f32 %1, %4, %5, %1 op_sel:[1,1,0] op_sel_hi:[0,1,1] neg_lo:[1,0,0]"
        : "=&v"(c0), "=&v"(c1) : "v"(a0), "v"(b0), "v"(a1), "v"(b1));
}

template <bool CONJ>
__device__ __forceinline__ v2f epow(float a2, float rh, float rl, float s) {
    float e = __builtin_amdgcn_exp2f(a2 * s);
    float x = __builtin_fmaf(rl, s, __builtin_amdgcn_fractf(rh * s));
    float cs = __builtin_amdgcn_cosf(x), sn = __builtin_amdgcn_sinf(x);
    v2f c;
    c[0] = e * cs;
    c[1] = CONJ ? -(e * sn) : (e * sn);
    return c;
}

#define H_COPY 4160
#define H_UNIT (4 * H_COPY)
#define R2_OFF (4 * H_UNIT)
#define TAB_PITCH 544
#define TAB_CH (64 * TAB_PITCH)
#define U_OFF R2_OFF
#define U_UNIT 4608
#define Y_OFF (U_OFF + 4 * U_UNIT)
#define G_OFF (Y_OFF + 4 * 4096)
#define T_OFF (R2_OFF + 2 * TAB_CH)
#define T_CH 5248
#define LDS_BYTES (T_OFF + 2 * T_CH)

#define WS_E 0
#define WS_INBOX 4096
#define WS_GATE (128 * 1024)
#define WS_UT (1024 * 1024)
#define WS_STAMPS (8 * 1024 * 1024)

#define SPIN_CAP (1u << 20)
#define RAW_BARRIER() do { asm volatile("s_waitcnt lgkmcnt(0)" ::: "memory"); __builtin_amdgcn_s_barrier(); asm volatile("" ::: "memory"); } while (0)

template <int CTRL>
__device__ __forceinline__ float dpp_add(float v) {
    int t = __builtin_amdgcn_update_dpp(0, __float_as_int(v), CTRL, 0xf, 0xf, true);
    return v + __int_as_float(t);
}

__host__ __device__ constexpr int pass_lo(int p) { return p == 0 ? 0 : (p == 1 ? 4 : 6); }
__host__ __device__ constexpr int pass_hi(int p) { return p == 0 ? 3 : (p == 1 ? 5 : 7); }

template <int S>
__device__ __forceinline__ void load_set(v8bf (&A)[8], v8bf (&B)[8], lptr ha, lptr ub) {
    constexpr int P = S / 8, r = S % 8, N = pass_hi(P) + 1;
#pragma unroll
    for (int i = 0; i < N; i++) {
        v4s lo = __builtin_amdgcn_ds_read_tr16_b64_v4i16((LAS v4s*)(ha + r * 64 + i * 512 + 8));
        v4s hi = __builtin_amdgcn_ds_read_tr16_b64_v4i16((LAS v4s*)(ha + r * 64 + i * 512));
        v8s ab = __builtin_shufflevector(lo, hi, 0, 1, 2, 3, 4, 5, 6, 7);
        A[i] = __builtin_bit_cast(v8bf, ab);
        B[i] = *(LAS v8bf*)(ub + (7 - r) * 64 + i * 512);
    }
}
template <int S>
__device__ __forceinline__ void mma_set(const v8bf (&A)[8], const v8bf (&B)[8], v4f (&acc)[8]) {
    constexpr int P = S / 8, LO = pass_lo(P), HI = pass_hi(P);
#pragma unroll
    for (int i = 0; i <= HI; i++)
#pragma unroll
        for (int q = 0; q <= HI; q++)
            if (i + q >= LO && i + q <= HI)
                acc[i + q] = __builtin_amdgcn_mfma_f32_16x16x32_bf16(A[i], B[q], acc[i + q], 0, 0, 0);
}
template <int S>
__device__ __forceinline__ void sched_step() {
    constexpr int P = S / 8;
    constexpr int M = (P == 0) ? 10 : (P == 1 ? 11 : 15);
    constexpr int PN = (S + 1) / 8;
    constexpr int R = (S + 1 < 24) ? 3 * (pass_hi(PN) + 1) : 0;
#pragma unroll
    for (int k = 0; k < M; k++) {
        __builtin_amdgcn_sched_group_barrier(0x8, 1, 0);
        if (R > 0) __builtin_amdgcn_sched_group_barrier(0x100, 2, 0);
    }
}
template <int S>
__device__ __forceinline__ void conv_steps(v8bf (&A0)[8], v8bf (&B0)[8], v8bf (&A1)[8], v8bf (&B1)[8], v4f (&acc)[8], lptr ha, lptr ub, lptr yb
) {
    if constexpr (S < 24) {
        if constexpr (S + 1 < 24) {
            if constexpr (S & 1) load_set<S + 1>(A0, B0, ha, ub); else load_set<S + 1>(A1, B1, ha, ub);
        }
        if constexpr (S & 1) mma_set<S>(A1, B1, acc); else mma_set<S>(A0, B0, acc);
        sched_step<S>();
        if constexpr (S % 8 == 7) {
            constexpr int P = S / 8;
#pragma unroll
            for (int t = pass_lo(P); t <= pass_hi(P); t++) {
                v2u o = {pk(acc[t][0], acc[t][1]), pk(acc[t][2], acc[t][3])};
                *(LAS v2u*)(yb + t * 512) = o;
            }
            RAW_BARRIER();
            STAMP(8 + P);
        }
        conv_steps<S + 1>(A0, B0, A1, B1, acc, ha, ub, yb
        );
    }
}

__device__ __forceinline__ void epi_rows(lptr sm, int t, u32* __restrict__ out32, int p, float dA0, float dA1, float dS0, float dS1) {
    const u32 ya0 = *(LAS u32*)(sm + Y_OFF + 0 * 4096 + t * 2), yb0 = *(LAS u32*)(sm + Y_OFF + 1 * 4096 + t * 2);
    const u32 ya1 = *(LAS u32*)(sm + Y_OFF + 2 * 4096 + t * 2), yb1 = *(LAS u32*)(sm + Y_OFF + 3 * 4096 + t * 2);
    const u32 kv0 = *(LAS u32*)(sm + U_OFF + 0 * U_UNIT + 512 + t * 2), x0 = *(LAS u32*)(sm + U_OFF + 1 * U_UNIT + 512 + t * 2);
    const u32 kv1 = *(LAS u32*)(sm + U_OFF + 2 * U_UNIT + 512 + t * 2), x1 = *(LAS u32*)(sm + U_OFF + 3 * U_UNIT + 512 + t * 2);
    const v2u gg = *(LAS v2u*)(sm + G_OFF + t * 4);
    {
        float g = bflo(gg[0]), g1 = bfhi(gg[0]);
        float a0 = bflo(x0) * (bflo(ya0) + bflo(kv0) * dA0), b0 = bflo(yb0) + bflo(x0) * dS0;
        float a1 = bflo(x1) * (bflo(ya1) + bflo(kv1) * dA1), b1 = bflo(yb1) + bflo(x1) * dS1;
        out32[t * 256 + p] = pk(g * a0 + g1 * b0, g * a1 + g1 * b1);
    }
    {
        float g = bflo(gg[1]), g1 = bfhi(gg[1]);
        float a0 = bfhi(x0) * (bfhi(ya0) + bfhi(kv0) * dA0), b0 = bfhi(yb0) + bfhi(x0) * dS0;
        float a1 = bfhi(x1) * (bfhi(ya1) + bfhi(kv1) * dA1), b1 = bfhi(yb1) + bfhi(x1) * dS1;
        out32[(t + 1) * 256 + p] = pk(g * a0 + g1 * b0, g * a1 + g1 * b1);
    }
}

__global__ void __launch_bounds__(512, 2)
fused_kernel(const u16* __restrict__ X, const u16* __restrict__ Kin, const u16* __restrict__ V, const u16* __restrict__ shiftk,
             const u16* __restrict__ hA, const u16* __restrict__ ssmD, const u16* __restrict__ Are, const u16* __restrict__ Aim,
             const u16* __restrict__ Bre, const u16* __restrict__ Bim, const u16* __restrict__ Cre, const u16* __restrict__ Cim,
             const u16* __restrict__ logdt, const u16* __restrict__ Dskip, const u16* __restrict__ gatew,
             char* __restrict__ ws, u32* __restrict__ out32) {
    __shared__ __attribute__((aligned(16))) char smem_[LDS_BYTES];
    const lptr sm = (lptr)smem_;
    const int tid = threadIdx.x;
    const int bid = blockIdx.x;
    const int lane = tid & 63;
    const int w = __builtin_amdgcn_readfirstlane(tid >> 6);
    const int dg = bid & 7, tg = bid >> 3;
    const int p = dg * 32 + tg;
    STAMP(0);

    const int cp = tid & 31, rb = tid >> 5;
    const int t0 = tg * 64 + rb * 4;
    const int c = dg * 64 + cp * 2;
    u32 kk[7], vv[4], xx[4];
    {
        const __amdgpu_buffer_rsrc_t k_rsrc = __builtin_amdgcn_make_buffer_rsrc((void*)Kin, 0, S_ * D_ * 2, 0x00020000);
        const u32 o = (u32)(t0 * D_ + c) * 2u;
        kk[0] = __builtin_amdgcn_raw_buffer_load_b32(k_rsrc, o - 3072u, 0, 0);
        kk[1] = __builtin_amdgcn_raw_buffer_load_b32(k_rsrc, o - 2048u, 0, 0);
        kk[2] = __builtin_amdgcn_raw_buffer_load_b32(k_rsrc, o - 1024u, 0, 0);
#pragma unroll
        for (int j = 0; j < 4; j++) kk[3 + j] = ldg<u32>(Kin, o + j * 1024);
#pragma unroll
        for (int j = 0; j < 4; j++) vv[j] = ldg<u32>(V, o + j * 1024);
#pragma unroll
        for (int j = 0; j < 4; j++) xx[j] = ldg<u32>(X, o + j * 1024);
    }
    const v4u sk = ldg<v4u>(shiftk, (u32)c * 8u);
    const int kch = (tid >> 7) & 1, kn = tid & 127;
    const int kd = p * 2 + kch;
    const u32 pi = (u32)(kd * N_ + kn) * 2u;
    const u16 p_ldt = ldg<u16>(logdt, (u32)kd * 2u);
    const u16 p_are = ldg<u16>(Are, pi), p_aim = ldg<u16>(Aim, pi), p_bre = ldg<u16>(Bre, pi), p_bim = ldg<u16>(Bim, pi), p_cre = ldg<u16>(Cre, pi), p_cim = ldg<u16>(Cim, pi);
    const u64 E = __hip_atomic_load((GAS u64*)(ws + WS_E), __ATOMIC_RELAXED, __HIP_MEMORY_SCOPE_AGENT);
    const int hch = tid >> 8, hck = tid & 255;
    const u32 ho = (u32)((p * 2 + hch) * S_ + hck * 8) * 2u;
    const v4u hd = ldg<v4u>(hA, ho);
    v2u hm = ldg<v2u>(hA, ho - (hck ? 8u : 0u));
    const int tgate = bid * 8 + w;
    const v4u gx = ldg<v4u>(X, (u32)(tgate * D_ + lane * 8) * 2u);
    const v4u gw = ldg<v4u>(gatew, (u32)lane * 16u);
    const u32 dAp = *(const u32*)(ssmD + p * 2), dSp = *(const u32*)(Dskip + p * 2);
    __builtin_amdgcn_sched_barrier(0);
    STAMP(13);

    {
        float s0[4] = {bflo(sk[0]), bfhi(sk[0]), bflo(sk[1]), bfhi(sk[1])};
        float s1[4] = {bflo(sk[2]), bfhi(sk[2]), bflo(sk[3]), bfhi(sk[3])};
        u32 kvp[4];
#pragma unroll
        for (int j = 0; j < 4; j++) {
            float a0 = bflo(kk[j]) * s0[3] + bflo(kk[j + 1]) * s0[2] + bflo(kk[j + 2]) * s0[1] + bflo(kk[j + 3]) * s0[0];
            float a1 = bfhi(kk[j]) * s1[3] + bfhi(kk[j + 1]) * s1[2] + bfhi(kk[j + 2]) * s1[1] + bfhi(kk[j + 3]) * s1[0];
            kvp[j] = pk(a0 * bflo(vv[j]), a1 * bfhi(vv[j]));
        }
        STAMP(1);
        const int sw = (rb ^ ((cp & 7) << 1)) * 8;
        const lptr base = sm + (cp * 2) * 128 + sw;
        v2u a, b;
        a[0] = __builtin_amdgcn_perm(kvp[1], kvp[0], 0x05040100); a[1] = __builtin_amdgcn_perm(kvp[3], kvp[2], 0x05040100);
        b[0] = __builtin_amdgcn_perm(kvp[1], kvp[0], 0x07060302); b[1] = __builtin_amdgcn_perm(kvp[3], kvp[2], 0x07060302);
        *(LAS v2u*)(base) = a; *(LAS v2u*)(base + 128) = b;
        a[0] = __builtin_amdgcn_perm(xx[1], xx[0], 0x05040100); a[1] = __builtin_amdgcn_perm(xx[3], xx[2], 0x05040100);
        b[0] = __builtin_amdgcn_perm(xx[1], xx[0], 0x07060302); b[1] = __builtin_amdgcn_perm(xx[3], xx[2], 0x07060302);
        *(LAS v2u*)(base + 8192) = a; *(LAS v2u*)(base + 8192 + 128) = b;
    }
    RAW_BARRIER();
    const __amdgpu_buffer_rsrc_t ut_rsrc = __builtin_amdgcn_make_buffer_rsrc(ws + WS_UT, 0, 2 * D_ * S_ * 2, 0x00020000);
    {
#pragma unroll
        for (int i = 0; i < 2; i++) {
            const int ch = (tid >> 3) & 63, ck = tid & 7;
            const int sw = (ck ^ ((ch >> 1) & 7)) * 16;
            v4u d = *(LAS v4u*)(sm + i * 8192 + ch * 128 + sw);
            __builtin_amdgcn_raw_buffer_store_b128(d, ut_rsrc, (u32)(((i * D_ + dg * 64 + ch) * S_ + tg * 64 + ck * 8) * 2), 0, 16);
        }
    }
    STAMP(2);

    {
        const float dt = rbf(expf(bf1(p_ldt)));
        const float a = dt * bf1(p_are), b = dt * bf1(p_aim);
        const float br = dt * bf1(p_bre), bi = dt * bf1(p_bim);
        const float cre = bf1(p_cre), cim = bf1(p_cim);
        v2f CB;
        CB[0] = cre * br - cim * bi;
        CB[1] = -(cre * bi + cim * br);
        const float a2 = a * 1.4426950408889634f;
        const float rh = b * 0.15915494309189535f;
        const float rl = __builtin_fmaf(b, 0.15915494309189535f, -rh) + b * 6.4206383e-9f;
        const lptr tab = sm + R2_OFF + kch * TAB_CH + kn * 4;
#define PUT(row, v) (*(LAS u32*)(tab + (row) * TAB_PITCH) = pk2(v))
        if (w < 4) {
            v2f E0 = epow<true>(a2, rh, rl, 1.f), E1 = epow<true>(a2, rh, rl, 2.f), E2 = epow<true>(a2, rh, rl, 4.f), E7 = epow<true>(a2, rh, rl, 128.f);
            v2f F1 = epow<false>(a2, rh, rl, 8.f), F2 = epow<false>(a2, rh, rl, 16.f), F4 = epow<false>(a2, rh, rl, 32.f), F8 = epow<false>(a2, rh, rl, 64.f);
            v2f P[8], F[8], t0_, t1_;
            P[0] = CB;
            cmul2(P[1], P[0], E0, P[2], P[0], E1);
            cmul2(P[3], P[1], E1, P[4], P[0], E2);
            cmul2(P[5], P[1], E2, P[6], P[2], E2);
            cmul2(P[7], P[3], E2, F[3], F1, F2);
            F[0][0] = 1.f; F[0][1] = 0.f; F[1] = F1; F[2] = F2; F[4] = F4;
            cmul2(F[5], F4, F1, F[6], F4, F2);
            cmul2(F[7], F4, F[3], t0_, F8, F1);
            PUT(48 + 9, t0_);
#pragma unroll
            for (int e = 0; e < 8; e += 2) {
                PUT(e, P[e]); PUT(e + 1, P[e + 1]);
                cmul2(t0_, P[e], E7, t1_, P[e + 1], E7);
                PUT(8 + e, t0_); PUT(9 + e, t1_);
            }
#pragma unroll
            for (int e = 0; e < 8; e++) PUT(48 + e, F[e]);
            PUT(48 + 8, F8);
#pragma unroll
            for (int e = 2; e < 8; e += 2) {
                cmul2(t0_, F8, F[e], t1_, F8, F[e + 1]);
                PUT(48 + 8 + e, t0_); PUT(48 + 9 + e, t1_);
            }
        } else {
            v2f E1 = epow<true>(a2, rh, rl, 2.f), E2 = epow<true>(a2, rh, rl, 4.f), E7 = epow<true>(a2, rh, rl, 128.f), E8 = epow<true>(a2, rh, rl, 256.f);
            v2f E9 = epow<true>(a2, rh, rl, 512.f), E10 = epow<true>(a2, rh, rl, 1024.f), E11 = epow<true>(a2, rh, rl, 2048.f);
            v2f P2, P4, P6, W3, R[4], Q0, Q1, T0, T1, T2, T3, t0_, t1_;
            cmul2(P2, CB, E1, P4, CB, E2);
            cmul2(P6, P2, E2, W3, E7, E8);
            cmul2(R[0], CB, E8, R[1], P2, E8);
            cmul2(R[2], P4, E8, R[3], P6, E8);
#pragma unroll
            for (int e = 0; e < 4; e += 2) {
                PUT(16 + e, R[e]); PUT(17 + e, R[e + 1]);
                cmul2(t0_, R[e], E7, t1_, R[e + 1], E7);
                PUT(20 + e, t0_); PUT(21 + e, t1_);
            }
            cmul2(Q0, CB, E9, Q1, P4, E9);
            PUT(24, Q0); PUT(28, Q1);
            cmul2(t0_, Q0, E7, t1_, Q1, E7); PUT(25, t0_); PUT(29, t1_);
            cmul2(t0_, Q0, E8, t1_, Q1, E8); PUT(26, t0_); PUT(30, t1_);
            cmul2(t0_, Q0, W3, t1_, Q1, W3); PUT(27, t0_); PUT(31, t1_);
            cmul2(T0, CB, E10, t0_, CB, E11); PUT(32, T0); PUT(40, t0_);
            cmul2(T1, T0, E7, T2, T0, E8); PUT(33, T1); PUT(34, T2);
            cmul2(T3, T0, W3, t0_, T0, E9); PUT(35, T3); PUT(36, t0_);
            cmul2(t0_, T1, E9, t1_, T2, E9); PUT(37, t0_); PUT(38, t1_);
            cmul2(t0_, T3, E9, t1_, T3, E9); PUT(39, t0_);
        }
    }
    STAMP(3);
    const u64 epoch = (u64)__builtin_amdgcn_readfirstlane((u32)E) + ((u64)__builtin_amdgcn_readfirstlane((u32)(E >> 32)) << 32) + 1ull;
    asm volatile("s_waitcnt vmcnt(0)" ::: "memory");
    RAW_BARRIER();
    STAMP(4);
    if (w == 0 && lane < 32) {
        GAS u64* inbox = (GAS u64*)(ws + WS_INBOX) + (dg * 32 + lane) * 32 + tg;
        __hip_atomic_store(inbox, epoch, __ATOMIC_RELAXED, __HIP_MEMORY_SCOPE_AGENT);
    }
    {
        float s = 0.f;
#pragma unroll
        for (int j = 0; j < 4; j++) {
            u32 pp = pk(bflo(gx[j]) * bflo(gw[j]), bfhi(gx[j]) * bfhi(gw[j]));
            s += bflo(pp) + bfhi(pp);
        }
        s = dpp_add<0x111>(s);
        s = dpp_add<0x112>(s);
        s = dpp_add<0x114>(s);
        s = dpp_add<0x118>(s);
        const int si = __float_as_int(s);
        float tot = __int_as_float(__builtin_amdgcn_readlane(si, 15)) + __int_as_float(__builtin_amdgcn_readlane(si, 31)) +
                    __int_as_float(__builtin_amdgcn_readlane(si, 47)) + __int_as_float(__builtin_amdgcn_readlane(si, 63));
        float z = rbf(tot);
        float g = 1.f / (1.f + __expf(-z));
        u32 gb = pk(g, g) & 0xffff;
        if (lane == 0) __hip_atomic_store((GAS u64*)(ws + WS_GATE) + tgate, (epoch << 16) | gb, __ATOMIC_RELAXED, __HIP_MEMORY_SCOPE_AGENT);
    }
    if (w < 6) {
        const int c2 = (w >= 3) ? 1 : 0, mt = w - c2 * 3;
        const lptr ga = sm + R2_OFF + c2 * TAB_CH + (mt * 16 + (lane & 15)) * TAB_PITCH + (lane >> 4) * 16;
        const lptr fb = sm + R2_OFF + c2 * TAB_CH + (48 + (lane & 15)) * TAB_PITCH + (lane >> 4) * 16;
        v4f acc = {0.f, 0.f, 0.f, 0.f};
#pragma unroll
        for (int s = 0; s < 8; s++) {
            v8bf A = *(LAS v8bf*)(ga + s * 64);
            v8bf B = *(LAS v8bf*)(fb + s * 64);
            acc = __builtin_amdgcn_mfma_f32_16x16x32_bf16(A, B, acc, 0, 0, 0);
        }
        const int g4 = lane >> 4;
        int base, step;
        if (mt == 0) { base = ((g4 >> 1) << 7) + ((g4 & 1) << 2); step = 1; }
        else if (mt == 1) { base = (g4 < 2) ? (256 + (g4 << 7)) : (512 + ((g4 & 1) << 2)); step = (g4 < 2) ? 2 : 128; }
        else { base = (g4 < 2) ? (1024 + (g4 << 9)) : (2048 + ((g4 & 1) << 3)); step = 128; }
        const lptr T = sm + T_OFF + c2 * T_CH + (base + 8 * (lane & 15)) * 2;
        const u32 o01 = pk(acc[0], acc[1]), o23 = pk(acc[2], acc[3]);
        *(LAS u16*)(T) = (u16)o01;
        *(LAS u16*)(T + step * 2) = (u16)(o01 >> 16);
        *(LAS u16*)(T + step * 4) = (u16)o23;
        *(LAS u16*)(T + step * 6) = (u16)(o23 >> 16);
    }
    {
        if (hck == 0) { hm[0] = 0; hm[1] = 0; }
        const lptr dst = sm + (hch * 2) * H_UNIT + 32 + hck * 16;
        const u32 s0 = __builtin_amdgcn_alignbit(hm[1], hm[0], 16), s1 = __builtin_amdgcn_alignbit(hd[0], hm[1], 16);
        const u32 s2 = __builtin_amdgcn_alignbit(hd[1], hd[0], 16), s3 = __builtin_amdgcn_alignbit(hd[2], hd[1], 16);
        const u32 s4 = __builtin_amdgcn_alignbit(hd[3], hd[2], 16);
        *(LAS v4u*)(dst) = hd;
        *(LAS v4u*)(dst + H_COPY) = (v4u){s1, s2, s3, s4};
        *(LAS v4u*)(dst + 2 * H_COPY) = (v4u){hm[1], hd[0], hd[1], hd[2]};
        *(LAS v4u*)(dst + 3 * H_COPY) = (v4u){s0, s1, s2, s3};
        if (tid < 32) *(LAS v4u*)(sm + (tid >> 3) * H_UNIT + ((tid >> 1) & 3) * H_COPY + (tid & 1) * 16) = (v4u){0, 0, 0, 0};
    }
    RAW_BARRIER();
    STAMP(5);
    {
        const lptr T = sm + T_OFF + hch * T_CH;
        const int l0 = hck * 8;
        u32 hv[11];
#pragma unroll
        for (int j = 0; j < 11; j++) {
            const int l = l0 - 3 + j;
            const u32 lv = (u32)rbf((float)(j < 3 ? max(l, 0) : l));
            const u32 x = *(LAS u16*)(T + lv * 2);
            hv[j] = (j < 3 && l < 0) ? 0u : x;
        }
        u32 e0 = hv[1] | (hv[2] << 16), e1 = hv[3] | (hv[4] << 16), e2 = hv[5] | (hv[6] << 16), e3 = hv[7] | (hv[8] << 16), e4 = hv[9] | (hv[10] << 16);
        u32 o0 = hv[0] | (hv[1] << 16), o1 = hv[2] | (hv[3] << 16), o2 = hv[4] | (hv[5] << 16), o3 = hv[6] | (hv[7] << 16), o4 = hv[8] | (hv[9] << 16);
        const lptr dst = sm + (hch * 2 + 1) * H_UNIT + 32 + hck * 16;
        *(LAS v4u*)(dst) = (v4u){e1, e2, e3, e4};
        *(LAS v4u*)(dst + H_COPY) = (v4u){o1, o2, o3, o4};
        *(LAS v4u*)(dst + 2 * H_COPY) = (v4u){e0, e1, e2, e3};
        *(LAS v4u*)(dst + 3 * H_COPY) = (v4u){o0, o1, o2, o3};
        if (tid < 128) *(LAS v4u*)(sm + U_OFF + (tid >> 5) * U_UNIT + (tid & 31) * 16) = (v4u){0, 0, 0, 0};
    }
    STAMP(6);
    {
        GAS u64* inbox = (GAS u64*)(ws + WS_INBOX) + p * 32 + w * 4 + (lane & 3);
        for (u32 spins = 0; spins < SPIN_CAP; spins++) {
            const u64 v = __hip_atomic_load(inbox, __ATOMIC_RELAXED, __HIP_MEMORY_SCOPE_AGENT);
            if (__all(v == epoch)) break;
            __builtin_amdgcn_s_sleep(1);
        }
        asm volatile("" ::: "memory");
        STAMP(7);
        v4u ud[2];
#pragma unroll
        for (int i = 0; i < 2; i++) {
            const int u = i * 2 + (lane >> 5);
            ud[i] = __builtin_amdgcn_raw_buffer_load_b128(ut_rsrc, (u32)((((u & 1) * D_ + p * 2 + (u >> 1)) * S_ + w * 256 + (lane & 31) * 8) * 2), 0, 16);
        }
#pragma unroll
        for (int i = 0; i < 2; i++) {
            const int u = i * 2 + (lane >> 5);
            *(LAS v4u*)(sm + U_OFF + u * U_UNIT + 512 + (w * 256 + (lane & 31) * 8) * 2) = ud[i];
        }
    }
    float dA0 = bflo(dAp), dA1 = bfhi(dAp), dS0 = bflo(dSp), dS1 = bfhi(dSp);
    asm volatile("" : "+v"(dA0), "+v"(dA1), "+v"(dS0), "+v"(dS1));
    RAW_BARRIER();
    STAMP(11);
    if (w < 4) {
        const int g4 = lane >> 4, n = lane & 15, q = (lane >> 2) & 3, pp = lane & 3;
        const lptr ha = sm + w * H_UNIT + q * H_COPY + 32 + 32 - 16 * g4 + 8 * pp - 8;
        const lptr ub = sm + U_OFF + w * U_UNIT + 512 + 32 * n + 16 * g4 - 32 - 7 * 64;
        const lptr yb = sm + Y_OFF + w * 4096 + 32 * n + 8 * g4;
        v8bf A0[8], B0[8], A1[8], B1[8];
        v4f acc[8];
#pragma unroll
        for (int i = 0; i < 8; i++) acc[i] = (v4f){0.f, 0.f, 0.f, 0.f};
        load_set<0>(A0, B0, ha, ub);
        conv_steps<0>(A0, B0, A1, B1, acc, ha, ub, yb
        );
    } else {
        const int e = tid - 256;
        {
            GAS u64* gr = (GAS u64*)(ws + WS_GATE) + e;
            const u64 tag = epoch & 0xFFFFFFFFFFFFull;
            u32 gv[8];
            for (u32 spins = 0; spins < SPIN_CAP; spins++) {
                bool ok = true;
#pragma unroll
                for (int i = 0; i < 8; i++) {
                    const u64 v = __hip_atomic_load(gr + i * 256, __ATOMIC_RELAXED, __HIP_MEMORY_SCOPE_AGENT);
                    gv[i] = (u32)v & 0xffff;
                    ok &= (v >> 16) == tag;
                }
                if (__all(ok)) break;
                __builtin_amdgcn_s_sleep(1);
            }
#pragma unroll
            for (int i = 0; i < 8; i++) {
                const float g = bf1((u16)gv[i]);
                *(LAS u32*)(sm + G_OFF + (e + i * 256) * 4) = gv[i] | (pk(0.f, 1.f - g) & 0xffff0000u);
            }
        }
        RAW_BARRIER();
        STAMP(8);
        if (bid == 0 && tid == 256) __hip_atomic_store((GAS u64*)(ws + WS_E), epoch, __ATOMIC_RELAXED, __HIP_MEMORY_SCOPE_AGENT);
        epi_rows(sm, e * 2, out32, p, dA0, dA1, dS0, dS1);
        epi_rows(sm, 512 + e * 2, out32, p, dA0, dA1, dS0, dS1);
        RAW_BARRIER();
        STAMP(9);
        epi_rows(sm, 1024 + e * 2, out32, p, dA0, dA1, dS0, dS1);
        RAW_BARRIER();
        STAMP(10);
        epi_rows(sm, 1536 + e * 2, out32, p, dA0, dA1, dS0, dS1);
        STAMP(12);
    }
}

extern "C" void kernel_launch(void* const* d_in, const int* in_sizes, int n_in,
                              void* d_out, int out_size, void* d_ws, size_t ws_size,
                              hipStream_t stream) {
    fused_kernel<<<256, 512, 0, stream>>>((const u16*)d_in[0], (const u16*)d_in[1], (const u16*)d_in[2], (const u16*)d_in[3],
                                          (const u16*)d_in[4], (const u16*)d_in[5], (const u16*)d_in[7], (const u16*)d_in[8],
                                          (const u16*)d_in[9], (const u16*)d_in[10], (const u16*)d_in[11], (const u16*)d_in[12],
                                          (const u16*)d_in[13], (const u16*)d_in[14], (const u16*)d_in[15], (char*)d_ws, (u32*)d_out);
}
```

```cpp
#include <hip/hip_runtime.h>
#include <stdint.h>

typedef unsigned short u16;
typedef unsigned int u32;
typedef unsigned long long u64;
typedef short v4s __attribute__((ext_vector_type(4)));
typedef short v8s __attribute__((ext_vector_type(8)));
typedef __bf16 v8bf __attribute__((ext_vector_type(8)));
typedef __bf16 v2bf __attribute__((ext_vector_type(2)));
typedef float v4f __attribute__((ext_vector_type(4)));
typedef float v2f __attribute__((ext_vector_type(2)));
typedef u32 v4u __attribute__((ext_vector_type(4)));
typedef u32 v2u __attribute__((ext_vector_type(2)));
#define LAS __attribute__((address_space(3)))
#define GAS __attribute__((address_space(1)))
typedef LAS char* lptr;

#define S_ 2048
#define D_ 512
#define N_ 128

#define STAMP(i) do { } while (0)

__device__ __forceinline__ float bflo(u32 v) { return __uint_as_float(v << 16); }
__device__ __forceinline__ float bfhi(u32 v) { return __uint_as_float(v & 0xffff0000u); }
__device__ __forceinline__ float bf1(u16 v) { return __uint_as_float(((u32)v) << 16); }
__device__ __forceinline__ u32 pk(float lo, float hi) {
    v2f x = {lo, hi};
    return __builtin_bit_cast(u32, __builtin_convertvector(x, v2bf));
}
__device__ __forceinline__ u32 pk2(v2f x) { return __builtin_bit_cast(u32, __builtin_convertvector(x, v2bf)); }
__device__ __forceinline__ u32 rot16(u32 v) { return __builtin_amdgcn_alignbit(v, v, 16); }
__device__ __forceinline__ float rbf(float f) { return bflo(pk(f, f)); }

template <typename T>
__device__ __forceinline__ T ldg(const void* base, u32 off) { return *(const GAS T*)((const GAS char*)base + off); }

__device__ __forceinline__ void cmul2(v2f& c0, v2f a0, v2f b0, v2f& c1, v2f a1, v2f b1) {
    asm("v_pk_mul_f32 %0, %2, %3 op_sel:[0,0] op_sel_hi:[1,0]\n\t"
        "v_pk_mul_f32 %1, %4, %5 op_sel:[0,0] op_sel_hi:[1,0]\n\t"
        "v_pk_fma_f32 %0, %2, %3, %0 op_sel:[1,1,0] op_sel_hi:[0,1,1] neg_lo:[1,0,0]\n\t"
        "v_pk_fma_f32 %1, %4, %5, %1 op_sel:[1,1,0] op_sel_hi:[0,1,1] neg_lo:[1,0,0]"
        : "=&v"(c0), "=&v"(c1) : "v"(a0), "v"(b0), "v"(a1), "v"(b1));
}

template <bool CONJ>
__device__ __forceinline__ v2f epow(float a2, float rh, float rl, float s) {
    float e = __builtin_amdgcn_exp2f(a2 * s);
    float x = __builtin_fmaf(rl, s, __builtin_amdgcn_fractf(rh * s));
    float cs = __builtin_amdgcn_cosf(x), sn = __builtin_amdgcn_sinf(x);
    v2f c;
    c[0] = e * cs;
    c[1] = CONJ ? -(e * sn) : (e * sn);
    return c;
}

#define H_COPY 4160
#define H_UNIT (4 * H_COPY)
#define R2_OFF (4 * H_UNIT)
#define TAB_PITCH 544
#define TAB_CH (64 * TAB_PITCH)
#define U_OFF R2_OFF
#define U_COPY 4640
#define U_UNIT (2 * U_COPY)
#define Y_OFF (U_OFF + 4 * U_UNIT)
#define T_OFF (R2_OFF + 2 * TAB_CH)
#define T_CH 5248
#define F_OFF (T_OFF + 2 * T_CH)
#define LDS_BYTES (F_OFF + 16)

#define WS_E 0
#define WS_INBOX 4096
#define WS_GATE (128 * 1024)
#define WS_UT (1024 * 1024)
#define WS_STAMPS (8 * 1024 * 1024)

#define SPIN_CAP (1u << 20)
#define RAW_BARRIER() do { asm volatile("s_waitcnt lgkmcnt(0)" ::: "memory"); __builtin_amdgcn_s_barrier(); asm volatile("" ::: "memory"); } while (0)

template <int CTRL>
__device__ __forceinline__ float dpp_add(float v) {
    int t = __builtin_amdgcn_update_dpp(0, __float_as_int(v), CTRL, 0xf, 0xf, true);
    return v + __int_as_float(t);
}

#define NSTEP 16
#define PASS_SPLIT 5
__host__ __device__ constexpr int pass_lo(int p) { return p == 0 ? 0 : PASS_SPLIT; }
__host__ __device__ constexpr int pass_hi(int p) { return p == 0 ? PASS_SPLIT - 1 : 7; }
__host__ __device__ constexpr int pass_nm(int p) {
    int c = 0;
    for (int i = 0; i <= pass_hi(p); i++) for (int q = 0; q <= pass_hi(p); q++) if (i + q >= pass_lo(p) && i + q <= pass_hi(p)) c++;
    return c;
}

template <int S>
__device__ __forceinline__ void load_set(v8bf (&A)[8], v8bf (&B)[8], lptr ha, lptr ub) {
    constexpr int P = S / 8, r = S % 8, N = pass_hi(P) + 1;
#pragma unroll
    for (int i = 0; i < N; i++) {
        A[i] = *(LAS v8bf*)(ha + (7 - r) * 64 + (7 - i) * 512);
        B[i] = *(LAS v8bf*)(ub + (7 - r) * 64 + i * 512);
    }
}
template <int S>
__device__ __forceinline__ void mma_set(const v8bf (&A)[8], const v8bf (&B)[8], v4f (&acc)[8]) {
    constexpr int P = S / 8, LO = pass_lo(P), HI = pass_hi(P);
#pragma unroll
    for (int i = 0; i <= HI; i++)
#pragma unroll
        for (int q = 0; q <= HI; q++)
            if (i + q >= LO && i + q <= HI)
                acc[i + q] = __builtin_amdgcn_mfma_f32_16x16x32_bf16(A[i], B[q], acc[i + q], 0, 0, 0);
}
template <int S>
__device__ __forceinline__ void sched_step() {
    constexpr int M = pass_nm(S / 8);
    constexpr int R = (S + 1 < NSTEP) ? 2 * (pass_hi((S + 1) / 8) + 1) : 0;
    constexpr int per = (R + M - 1) / M;
#pragma unroll
    for (int k = 0; k < M; k++) {
        __builtin_amdgcn_sched_group_barrier(0x8, 1, 0);
        if (R > 0) __builtin_amdgcn_sched_group_barrier(0x100, per, 0);
    }
}
template <int S>
__device__ __forceinline__ void conv_steps(v8bf (&A0)[8], v8bf (&B0)[8], v8bf (&A1)[8], v8bf (&B1)[8], v4f (&acc)[8], lptr ha, lptr ub, lptr yb, lptr flags
) {
    if constexpr (S < NSTEP) {
        if constexpr (S + 1 < NSTEP) {
            if constexpr (S & 1) load_set<S + 1>(A0, B0, ha, ub); else load_set<S + 1>(A1, B1, ha, ub);
        }
        if constexpr (S & 1) mma_set<S>(A1, B1, acc); else mma_set<S>(A0, B0, acc);
        sched_step<S>();
        if constexpr (S % 8 == 7) {
            constexpr int P = S / 8;
#pragma unroll
            for (int t = pass_lo(P); t <= pass_hi(P); t++) {
                v2u o = {pk(acc[t][0], acc[t][1]), pk(acc[t][2], acc[t][3])};
                *(LAS v2u*)(yb + t * 512) = o;
            }
            asm volatile("s_waitcnt lgkmcnt(0)" ::: "memory");
            if ((threadIdx.x & 63) == 0) __hip_atomic_fetch_add((LAS u32*)(flags + P * 4), 1u, __ATOMIC_RELAXED, __HIP_MEMORY_SCOPE_WORKGROUP);
            STAMP(8 + P);
        }
        conv_steps<S + 1>(A0, B0, A1, B1, acc, ha, ub, yb, flags
        );
    }
}

__device__ __forceinline__ void wait_lds(lptr word, u32 want) {
    for (u32 spins = 0; spins < SPIN_CAP; spins++) {
        const u32 v = __hip_atomic_load((LAS u32*)word, __ATOMIC_RELAXED, __HIP_MEMORY_SCOPE_WORKGROUP);
        if (__builtin_amdgcn_readfirstlane(v) >= want) break;
        __builtin_amdgcn_s_sleep(2);
    }
    asm volatile("" ::: "memory");
}

__device__ __forceinline__ void epi_chunk(lptr sm, int T0, const u32 (&gv)[8], u32* __restrict__ out32, int p, float dA0, float dA1, float dS0, float dS1) {
    const v4u ya0 = *(LAS v4u*)(sm + Y_OFF + 0 * 4096 + T0 * 2), yb0 = *(LAS v4u*)(sm + Y_OFF + 1 * 4096 + T0 * 2);
    const v4u ya1 = *(LAS v4u*)(sm + Y_OFF + 2 * 4096 + T0 * 2), yb1 = *(LAS v4u*)(sm + Y_OFF + 3 * 4096 + T0 * 2);
    const lptr ub = sm + U_OFF + U_COPY + 512 + T0 * 2;
    const v4u kv0 = *(LAS v4u*)(ub + 0 * U_UNIT), x0 = *(LAS v4u*)(ub + 1 * U_UNIT);
    const v4u kv1 = *(LAS v4u*)(ub + 2 * U_UNIT), x1 = *(LAS v4u*)(ub + 3 * U_UNIT);
    u32* o = out32 + (u32)(T0 * 256 + p);
#pragma unroll
    for (int e = 0; e < 8; e++) {
        const int yd = (e & 1) * 2 + (e >> 2);
        const bool yh = (e >> 1) & 1;
        const int nd = e >> 1;
        const bool nh = e & 1;
        const float fya0 = yh ? bfhi(ya0[yd]) : bflo(ya0[yd]), fyb0 = yh ? bfhi(yb0[yd]) : bflo(yb0[yd]);
        const float fya1 = yh ? bfhi(ya1[yd]) : bflo(ya1[yd]), fyb1 = yh ? bfhi(yb1[yd]) : bflo(yb1[yd]);
        const float fkv0 = nh ? bfhi(kv0[nd]) : bflo(kv0[nd]), fx0 = nh ? bfhi(x0[nd]) : bflo(x0[nd]);
        const float fkv1 = nh ? bfhi(kv1[nd]) : bflo(kv1[nd]), fx1 = nh ? bfhi(x1[nd]) : bflo(x1[nd]);
        const float g = bflo(gv[e]), g1 = bfhi(gv[e]);
        const float a0 = fx0 * (fya0 + fkv0 * dA0), b0 = fyb0 + fx0 * dS0;
        const float a1 = fx1 * (fya1 + fkv1 * dA1), b1 = fyb1 + fx1 * dS1;
        o[e * 256] = pk(g * a0 + g1 * b0, g * a1 + g1 * b1);
    }
}

__global__ void __launch_bounds__(512, 2)
fused_kernel(const u16* __restrict__ X, const u16* __restrict__ Kin, const u16* __restrict__ V, const u16* __restrict__ shiftk,
             const u16* __restrict__ hA, const u16* __restrict__ ssmD, const u16* __restrict__ Are, const u16* __restrict__ Aim,
             const u16* __restrict__ Bre, const u16* __restrict__ Bim, const u16* __restrict__ Cre, const u16* __restrict__ Cim,
             const u16* __restrict__ logdt, const u16* __restrict__ Dskip, const u16* __restrict__ gatew,
             char* __restrict__ ws, u32* __restrict__ out32) {
    __shared__ __attribute__((aligned(16))) char smem_[LDS_BYTES];
    const lptr sm = (lptr)smem_;
    const int tid = threadIdx.x;
    const int bid = blockIdx.x;
    const int lane = tid & 63;
    const int w = __builtin_amdgcn_readfirstlane(tid >> 6);
    const int dg = bid & 7, tg = bid >> 3;
    const int p = dg * 32 + tg;
    STAMP(0);

    const int cp = tid & 31, rb = tid >> 5;
    const int t0 = tg * 64 + rb * 4;
    const int c = dg * 64 + cp * 2;
    u32 kk[7], vv[4], xx[4];
    {
        const __amdgpu_buffer_rsrc_t k_rsrc = __builtin_amdgcn_make_buffer_rsrc((void*)Kin, 0, S_ * D_ * 2, 0x00020000);
        const u32 o = (u32)(t0 * D_ + c) * 2u;
        kk[0] = __builtin_amdgcn_raw_buffer_load_b32(k_rsrc, o - 3072u, 0, 0);
        kk[1] = __builtin_amdgcn_raw_buffer_load_b32(k_rsrc, o - 2048u, 0, 0);
        kk[2] = __builtin_amdgcn_raw_buffer_load_b32(k_rsrc, o - 1024u, 0, 0);
#pragma unroll
        for (int j = 0; j < 4; j++) kk[3 + j] = ldg<u32>(Kin, o + j * 1024);
#pragma unroll
        for (int j = 0; j < 4; j++) vv[j] = ldg<u32>(V, o + j * 1024);
#pragma unroll
        for (int j = 0; j < 4; j++) xx[j] = ldg<u32>(X, o + j * 1024);
    }
    const v4u sk = ldg<v4u>(shiftk, (u32)c * 8u);
    const int kch = (tid >> 7) & 1, kn = tid & 127;
    const int kd = p * 2 + kch;
    const u32 pi = (u32)(kd * N_ + kn) * 2u;
    const u16 p_ldt = ldg<u16>(logdt, (u32)kd * 2u);
    const u16 p_are = ldg<u16>(Are, pi), p_aim = ldg<u16>(Aim, pi), p_bre = ldg<u16>(Bre, pi), p_bim = ldg<u16>(Bim, pi), p_cre = ldg<u16>(Cre, pi), p_cim = ldg<u16>(Cim, pi);
    const u64 E = __hip_atomic_load((GAS u64*)(ws + WS_E), __ATOMIC_RELAXED, __HIP_MEMORY_SCOPE_AGENT);
    const int hch = tid >> 8, hck = tid & 255;
    const u32 ho = (u32)((p * 2 + hch) * S_ + hck * 8) * 2u;
    const v4u hd = ldg<v4u>(hA, ho);
    v4u hm = ldg<v4u>(hA, ho - (hck ? 16u : 0u));
    const int tgate = bid * 8 + w;
    const v4u gx = ldg<v4u>(X, (u32)(tgate * D_ + lane * 8) * 2u);
    const v4u gw = ldg<v4u>(gatew, (u32)lane * 16u);
    const u32 dAp = *(const u32*)(ssmD + p * 2), dSp = *(const u32*)(Dskip + p * 2);
    __builtin_amdgcn_sched_barrier(0);
    STAMP(13);

    if (tid < 4) *(LAS u32*)(sm + F_OFF + tid * 4) = 0;
    {
        float s0[4] = {bflo(sk[0]), bfhi(sk[0]), bflo(sk[1]), bfhi(sk[1])};
        float s1[4] = {bflo(sk[2]), bfhi(sk[2]), bflo(sk[3]), bfhi(sk[3])};
        u32 kvp[4];
#pragma unroll
        for (int j = 0; j < 4; j++) {
            float a0 = bflo(kk[j]) * s0[3] + bflo(kk[j + 1]) * s0[2] + bflo(kk[j + 2]) * s0[1] + bflo(kk[j + 3]) * s0[0];
            float a1 = bfhi(kk[j]) * s1[3] + bfhi(kk[j + 1]) * s1[2] + bfhi(kk[j + 2]) * s1[1] + bfhi(kk[j + 3]) * s1[0];
            kvp[j] = pk(a0 * bflo(vv[j]), a1 * bfhi(vv[j]));
        }
        STAMP(1);
        const int sw = (rb ^ ((cp & 7) << 1)) * 8;
        const lptr base = sm + (cp * 2) * 128 + sw;
        v2u a, b;
        a[0] = __builtin_amdgcn_perm(kvp[1], kvp[0], 0x05040100); a[1] = __builtin_amdgcn_perm(kvp[3], kvp[2], 0x05040100);
        b[0] = __builtin_amdgcn_perm(kvp[1], kvp[0], 0x07060302); b[1] = __builtin_amdgcn_perm(kvp[3], kvp[2], 0x07060302);
        *(LAS v2u*)(base) = a; *(LAS v2u*)(base + 128) = b;
        a[0] = __builtin_amdgcn_perm(xx[1], xx[0], 0x05040100); a[1] = __builtin_amdgcn_perm(xx[3], xx[2], 0x05040100);
        b[0] = __builtin_amdgcn_perm(xx[1], xx[0], 0x07060302); b[1] = __builtin_amdgcn_perm(xx[3], xx[2], 0x07060302);
        *(LAS v2u*)(base + 8192) = a; *(LAS v2u*)(base + 8192 + 128) = b;
    }
    RAW_BARRIER();
    const __amdgpu_buffer_rsrc_t ut_rsrc = __builtin_amdgcn_make_buffer_rsrc(ws + WS_UT, 0, 2 * D_ * S_ * 2, 0x00020000);
    {
#pragma unroll
        for (int i = 0; i < 2; i++) {
            const int ch = (tid >> 3) & 63, ck = tid & 7;
            const int sw = (ck ^ ((ch >> 1) & 7)) * 16;
            v4u d = *(LAS v4u*)(sm + i * 8192 + ch * 128 + sw);
            __builtin_amdgcn_raw_buffer_store_b128(d, ut_rsrc, (u32)(((i * D_ + dg * 64 + ch) * S_ + tg * 64 + ck * 8) * 2), 0, 16);
        }
    }
    STAMP(2);

    {
        const float dt = rbf(expf(bf1(p_ldt)));
        const float a = dt * bf1(p_are), b = dt * bf1(p_aim);
        const float br = dt * bf1(p_bre), bi = dt * bf1(p_bim);
        const float cre = bf1(p_cre), cim = bf1(p_cim);
        v2f CB;
        CB[0] = cre * br - cim * bi;
        CB[1] = -(cre * bi + cim * br);
        const float a2 = a * 1.4426950408889634f;
        const float rh = b * 0.15915494309189535f;
        const float rl = __builtin_fmaf(b, 0.15915494309189535f, -rh) + b * 6.4206383e-9f;
        const lptr tab = sm + R2_OFF + kch * TAB_CH + kn * 4;
#define PUT(row, v) (*(LAS u32*)(tab + (row) * TAB_PITCH) = pk2(v))
        if (w < 4) {
            v2f E0 = epow<true>(a2, rh, rl, 1.f), E1 = epow<true>(a2, rh, rl, 2.f), E2 = epow<true>(a2, rh, rl, 4.f), E7 = epow<true>(a2, rh, rl, 128.f);
            v2f F1 = epow<false>(a2, rh, rl, 8.f), F2 = epow<false>(a2, rh, rl, 16.f), F4 = epow<false>(a2, rh, rl, 32.f), F8 = epow<false>(a2, rh, rl, 64.f);
            v2f P[8], F[8], t0_, t1_;
            P[0] = CB;
            cmul2(P[1], P[0], E0, P[2], P[0], E1);
            cmul2(P[3], P[1], E1, P[4], P[0], E2);
            cmul2(P[5], P[1], E2, P[6], P[2], E2);
            cmul2(P[7], P[3], E2, F[3], F1, F2);
            F[0][0] = 1.f; F[0][1] = 0.f; F[1] = F1; F[2] = F2; F[4] = F4;
            cmul2(F[5], F4, F1, F[6], F4, F2);
            cmul2(F[7], F4, F[3], t0_, F8, F1);
            PUT(48 + 9, t0_);
#pragma unroll
            for (int e = 0; e < 8; e += 2) {
                PUT(e, P[e]); PUT(e + 1, P[e + 1]);
                cmul2(t0_, P[e], E7, t1_, P[e + 1], E7);
                PUT(8 + e, t0_); PUT(9 + e, t1_);
            }
#pragma unroll
            for (int e = 0; e < 8; e++) PUT(48 + e, F[e]);
            PUT(48 + 8, F8);
#pragma unroll
            for (int e = 2; e < 8; e += 2) {
                cmul2(t0_, F8, F[e], t1_, F8, F[e + 1]);
                PUT(48 + 8 + e, t0_); PUT(48 + 9 + e, t1_);
            }
        } else {
            v2f E1 = epow<true>(a2, rh, rl, 2.f), E2 = epow<true>(a2, rh, rl, 4.f), E7 = epow<true>(a2, rh, rl, 128.f), E8 = epow<true>(a2, rh, rl, 256.f);
            v2f E9 = epow<true>(a2, rh, rl, 512.f), E10 = epow<true>(a2, rh, rl, 1024.f), E11 = epow<true>(a2, rh, rl, 2048.f);
            v2f P2, P4, P6, W3, R[4], Q0, Q1, T0, T1, T2, T3, t0_, t1_;
            cmul2(P2, CB, E1, P4, CB, E2);
            cmul2(P6, P2, E2, W3, E7, E8);
            cmul2(R[0], CB, E8, R[1], P2, E8);
            cmul2(R[2], P4, E8, R[3], P6, E8);
#pragma unroll
            for (int e = 0; e < 4; e += 2) {
                PUT(16 + e, R[e]); PUT(17 + e, R[e + 1]);
                cmul2(t0_, R[e], E7, t1_, R[e + 1], E7);
                PUT(20 + e, t0_); PUT(21 + e, t1_);
            }
            cmul2(Q0, CB, E9, Q1, P4, E9);
            PUT(24, Q0); PUT(28, Q1);
            cmul2(t0_, Q0, E7, t1_, Q1, E7); PUT(25, t0_); PUT(29, t1_);
            cmul2(t0_, Q0, E8, t1_, Q1, E8); PUT(26, t0_); PUT(30, t1_);
            cmul2(t0_, Q0, W3, t1_, Q1, W3); PUT(27, t0_); PUT(31, t1_);
            cmul2(T0, CB, E10, t0_, CB, E11); PUT(32, T0); PUT(40, t0_);
            cmul2(T1, T0, E7, T2, T0, E8); PUT(33, T1); PUT(34, T2);
            cmul2(T3, T0, W3, t0_, T0, E9); PUT(35, T3); PUT(36, t0_);
            cmul2(t0_, T1, E9, t1_, T2, E9); PUT(37, t0_); PUT(38, t1_);
            cmul2(t0_, T3, E9, t1_, T3, E9); PUT(39, t0_);
        }
    }
    STAMP(3);
    const u64 epoch = (u64)__builtin_amdgcn_readfirstlane((u32)E) + ((u64)__builtin_amdgcn_readfirstlane((u32)(E >> 32)) << 32) + 1ull;
    asm volatile("s_waitcnt vmcnt(0)" ::: "memory");
    RAW_BARRIER();
    STAMP(4);
    if (w == 0 && lane < 32) {
        GAS u64* inbox = (GAS u64*)(ws + WS_INBOX) + (dg * 32 + lane) * 32 + tg;
        __hip_atomic_store(inbox, epoch, __ATOMIC_RELAXED, __HIP_MEMORY_SCOPE_AGENT);
    }
    {
        float s = 0.f;
#pragma unroll
        for (int j = 0; j < 4; j++) {
            u32 pp = pk(bflo(gx[j]) * bflo(gw[j]), bfhi(gx[j]) * bfhi(gw[j]));
            s += bflo(pp) + bfhi(pp);
        }
        s = dpp_add<0x111>(s);
        s = dpp_add<0x112>(s);
        s = dpp_add<0x114>(s);
        s = dpp_add<0x118>(s);
        const int si = __float_as_int(s);
        float tot = __int_as_float(__builtin_amdgcn_readlane(si, 15)) + __int_as_float(__builtin_amdgcn_readlane(si, 31)) +
                    __int_as_float(__builtin_amdgcn_readlane(si, 47)) + __int_as_float(__builtin_amdgcn_readlane(si, 63));
        float z = rbf(tot);
        float g = 1.f / (1.f + __expf(-z));
        u32 gb = pk(g, g) & 0xffff;
        if (lane == 0) __hip_atomic_store((GAS u64*)(ws + WS_GATE) + tgate, (epoch << 16) | gb, __ATOMIC_RELAXED, __HIP_MEMORY_SCOPE_AGENT);
    }
    if (w < 6) {
        const int c2 = (w >= 3) ? 1 : 0, mt = w - c2 * 3;
        const lptr ga = sm + R2_OFF + c2 * TAB_CH + (mt * 16 + (lane & 15)) * TAB_PITCH + (lane >> 4) * 16;
        const lptr fb = sm + R2_OFF + c2 * TAB_CH + (48 + (lane & 15)) * TAB_PITCH + (lane >> 4) * 16;
        v4f acc = {0.f, 0.f, 0.f, 0.f};
#pragma unroll
        for (int s = 0; s < 8; s++) {
            v8bf A = *(LAS v8bf*)(ga + s * 64);
            v8bf B = *(LAS v8bf*)(fb + s * 64);
            acc = __builtin_amdgcn_mfma_f32_16x16x32_bf16(A, B, acc, 0, 0, 0);
        }
        const int g4 = lane >> 4;
        int base, step;
        if (mt == 0) { base = ((g4 >> 1) << 7) + ((g4 & 1) << 2); step = 1; }
        else if (mt == 1) { base = (g4 < 2) ? (256 + (g4 << 7)) : (512 + ((g4 & 1) << 2)); step = (g4 < 2) ? 2 : 128; }
        else { base = (g4 < 2) ? (1024 + (g4 << 9)) : (2048 + ((g4 & 1) << 3)); step = 128; }
        const lptr T = sm + T_OFF + c2 * T_CH + (base + 8 * (lane & 15)) * 2;
        const u32 o01 = pk(acc[0], acc[1]), o23 = pk(acc[2], acc[3]);
        *(LAS u16*)(T) = (u16)o01;
        *(LAS u16*)(T + step * 2) = (u16)(o01 >> 16);
        *(LAS u16*)(T + step * 4) = (u16)o23;
        *(LAS u16*)(T + step * 6) = (u16)(o23 >> 16);
    }
    {
        if (hck == 0) { hm[1] = 0; hm[2] = 0; hm[3] = 0; }
        u32 r[7];
        r[0] = rot16(hd[3]); r[1] = rot16(hd[2]); r[2] = rot16(hd[1]); r[3] = rot16(hd[0]);
        r[4] = rot16(hm[3]); r[5] = rot16(hm[2]); r[6] = rot16(hm[1]);
        const lptr dst = sm + (hch * 2) * H_UNIT + (255 - hck) * 16;
        *(LAS v4u*)(dst) = (v4u){r[3], r[4], r[5], r[6]};
        *(LAS v4u*)(dst + H_COPY) = (v4u){r[2], r[3], r[4], r[5]};
        *(LAS v4u*)(dst + 2 * H_COPY) = (v4u){r[1], r[2], r[3], r[4]};
        *(LAS v4u*)(dst + 3 * H_COPY) = (v4u){r[0], r[1], r[2], r[3]};
        if (tid < 48) {
            const int uc = tid / 3, ck = tid - uc * 3;
            *(LAS v4u*)(sm + uc * H_COPY + (256 + ck) * 16) = (v4u){0, 0, 0, 0};
        }
    }
    RAW_BARRIER();
    STAMP(5);
    {
        const lptr T = sm + T_OFF + hch * T_CH;
        const int l0 = hck * 8;
        u32 hv[14];
#pragma unroll
        for (int j = 0; j < 14; j++) {
            const int l = l0 - 6 + j;
            const u32 lv = (u32)rbf((float)(j < 6 ? max(l, 0) : l));
            const u32 x = *(LAS u16*)(T + lv * 2);
            hv[j] = (j < 6 && l < 0) ? 0u : x;
        }
        u32 r[7];
#pragma unroll
        for (int i = 0; i < 7; i++) r[i] = hv[13 - 2 * i] | (hv[12 - 2 * i] << 16);
        const lptr dst = sm + (hch * 2 + 1) * H_UNIT + (255 - hck) * 16;
        *(LAS v4u*)(dst) = (v4u){r[3], r[4], r[5], r[6]};
        *(LAS v4u*)(dst + H_COPY) = (v4u){r[2], r[3], r[4], r[5]};
        *(LAS v4u*)(dst + 2 * H_COPY) = (v4u){r[1], r[2], r[3], r[4]};
        *(LAS v4u*)(dst + 3 * H_COPY) = (v4u){r[0], r[1], r[2], r[3]};
        if (tid < 256) *(LAS v4u*)(sm + U_OFF + (tid >> 5) * U_COPY + (tid & 31) * 16) = (v4u){0, 0, 0, 0};
    }
    STAMP(6);
    {
        const int pidx = w * 4 - 1 + min(lane, 4);
        GAS u64* inbox = (GAS u64*)(ws + WS_INBOX) + p * 32 + max(pidx, 0);
        for (u32 spins = 0; spins < SPIN_CAP; spins++) {
            const u64 v = __hip_atomic_load(inbox, __ATOMIC_RELAXED, __HIP_MEMORY_SCOPE_AGENT);
            if (__all(v == epoch)) break;
            __builtin_amdgcn_s_sleep(1);
        }
        asm volatile("" ::: "memory");
        STAMP(7);
        v4u ud[2];
        u32 up[2];
#pragma unroll
        for (int i = 0; i < 2; i++) {
            const int u = i * 2 + (lane >> 5);
            const int t = w * 256 + (lane & 31) * 8;
            const u32 off = (u32)((((u & 1) * D_ + p * 2 + (u >> 1)) * S_ + t) * 2);
            ud[i] = __builtin_amdgcn_raw_buffer_load_b128(ut_rsrc, off, 0, 16);
            up[i] = __builtin_amdgcn_raw_buffer_load_b32(ut_rsrc, t ? off - 4u : off, 0, 16);
        }
#pragma unroll
        for (int i = 0; i < 2; i++) {
            const int u = i * 2 + (lane >> 5);
            const int t = w * 256 + (lane & 31) * 8;
            const lptr dst = sm + U_OFF + u * U_UNIT + 512 + t * 2;
            const u32 prev = t ? up[i] : 0u;
            *(LAS v4u*)(dst + U_COPY) = ud[i];
            *(LAS v4u*)(dst) = (v4u){__builtin_amdgcn_alignbit(ud[i][0], prev, 16), __builtin_amdgcn_alignbit(ud[i][1], ud[i][0], 16),
                                     __builtin_amdgcn_alignbit(ud[i][2], ud[i][1], 16), __builtin_amdgcn_alignbit(ud[i][3], ud[i][2], 16)};
        }
    }
    float dA0 = bflo(dAp), dA1 = bfhi(dAp), dS0 = bflo(dSp), dS1 = bfhi(dSp);
    asm volatile("" : "+v"(dA0), "+v"(dA1), "+v"(dS0), "+v"(dS1));
    RAW_BARRIER();
    STAMP(11);
    if (w < 4) {
        const int g4 = lane >> 4, n = lane & 15;
        const lptr ha = sm + w * H_UNIT + (n & 3) * H_COPY + 48 - 16 * (n >> 2) + 16 * g4;
        const lptr ub = sm + U_OFF + w * U_UNIT + (n & 1) * U_COPY + 512 + 64 * (n >> 1) + 16 * g4 - 7 * 64;
        const lptr yb = sm + Y_OFF + w * 4096 + 64 * (n >> 1) + 16 * g4 + 8 * (n & 1);
        v8bf A0[8], B0[8], A1[8], B1[8];
        v4f acc[8];
#pragma unroll
        for (int i = 0; i < 8; i++) acc[i] = (v4f){0.f, 0.f, 0.f, 0.f};
        load_set<0>(A0, B0, ha, ub);
        conv_steps<0>(A0, B0, A1, B1, acc, ha, ub, yb, sm + F_OFF
        );
    } else {
        const int ck = tid - 256;
        u32 gv[8];
        {
            GAS u64* gr = (GAS u64*)(ws + WS_GATE) + ck * 8;
            const u64 tag = epoch & 0xFFFFFFFFFFFFull;
            for (u32 spins = 0; spins < SPIN_CAP; spins++) {
                bool ok = true;
#pragma unroll
                for (int i = 0; i < 8; i++) {
                    const u64 v = __hip_atomic_load(gr + i, __ATOMIC_RELAXED, __HIP_MEMORY_SCOPE_AGENT);
                    gv[i] = (u32)v & 0xffff;
                    ok &= (v >> 16) == tag;
                }
                if (__all(ok)) break;
                __builtin_amdgcn_s_sleep(1);
            }
#pragma unroll
            for (int i = 0; i < 8; i++) gv[i] |= pk(0.f, 1.f - bf1((u16)gv[i])) & 0xffff0000u;
        }
        if (bid == 0 && lane == 0) {
            const u32 c4 = __hip_atomic_fetch_add((LAS u32*)(sm + F_OFF + 8), 1u, __ATOMIC_RELAXED, __HIP_MEMORY_SCOPE_WORKGROUP);
            if (c4 == 3) __hip_atomic_store((GAS u64*)(ws + WS_E), epoch, __ATOMIC_RELAXED, __HIP_MEMORY_SCOPE_AGENT);
        }
        STAMP(8);
        if (w < 7) {
            wait_lds(sm + F_OFF, 4);
            STAMP(9);
            if (ck < PASS_SPLIT * 32) epi_chunk(sm, ck * 8, gv, out32, p, dA0, dA1, dS0, dS1);
        }
        if (w >= 6) {
            wait_lds(sm + F_OFF + 4, 4);
            STAMP(10);
            if (ck >= PASS_SPLIT * 32) epi_chunk(sm, ck * 8, gv, out32, p, dA0, dA1, dS0, dS1);
        }
        STAMP(12);
    }
}

extern "C" void kernel_launch(void* const* d_in, const int* in_sizes, int n_in,
                              void* d_out, int out_size, void* d_ws, size_t ws_size,
                              hipStream_t stream) {
    fused_kernel<<<256, 512, 0, stream>>>((const u16*)d_in[0], (const u16*)d_in[1], (const u16*)d_in[2], (const u16*)d_in[3],
                                          (const u16*)d_in[4], (const u16*)d_in[5], (const u16*)d_in[7], (const u16*)d_in[8],
                                          (const u16*)d_in[9], (const u16*)d_in[10], (const u16*)d_in[11], (const u16*)d_in[12],
                                          (const u16*)d_in[13], (const u16*)d_in[14], (const u16*)d_in[15], (char*)d_ws, (u32*)d_out);
}
```

```cpp
#define AMPI 13
#include <hip/hip_runtime.h>
#include <stdint.h>

typedef unsigned short u16;
typedef unsigned int u32;
typedef unsigned long long u64;
typedef short v4s __attribute__((ext_vector_type(4)));
typedef short v8s __attribute__((ext_vector_type(8)));
typedef __bf16 v8bf __attribute__((ext_vector_type(8)));
typedef __bf16 v2bf __attribute__((ext_vector_type(2)));
typedef float v4f __attribute__((ext_vector_type(4)));
typedef float v2f __attribute__((ext_vector_type(2)));
typedef u32 v4u __attribute__((ext_vector_type(4)));
typedef u32 v2u __attribute__((ext_vector_type(2)));
#define LAS __attribute__((address_space(3)))
#define GAS __attribute__((address_space(1)))
typedef LAS char* lptr;

#define S_ 2048
#define D_ 512
#define N_ 128

#define STAMP(i) do { if ((i) == 0 || (i) == AMPI) { __builtin_amdgcn_sched_barrier(0); u64 _t = __builtin_amdgcn_s_memrealtime(); asm volatile("s_waitcnt lgkmcnt(0)" ::: "memory"); if ((i) == 0) amp_t0 = _t; else amp_t1 = _t; __builtin_amdgcn_sched_barrier(0); } } while (0)

__device__ __forceinline__ float bflo(u32 v) { return __uint_as_float(v << 16); }
__device__ __forceinline__ float bfhi(u32 v) { return __uint_as_float(v & 0xffff0000u); }
__device__ __forceinline__ float bf1(u16 v) { return __uint_as_float(((u32)v) << 16); }
__device__ __forceinline__ u32 pk(float lo, float hi) {
    v2f x = {lo, hi};
    return __builtin_bit_cast(u32, __builtin_convertvector(x, v2bf));
}
__device__ __forceinline__ u32 pk2(v2f x) { return __builtin_bit_cast(u32, __builtin_convertvector(x, v2bf)); }
__device__ __forceinline__ u32 rot16(u32 v) { return __builtin_amdgcn_alignbit(v, v, 16); }
__device__ __forceinline__ float rbf(float f) { return bflo(pk(f, f)); }

template <typename T>
__device__ __forceinline__ T ldg(const void* base, u32 off) { return *(const GAS T*)((const GAS char*)base + off); }

__device__ __forceinline__ void cmul2(v2f& c0, v2f a0, v2f b0, v2f& c1, v2f a1, v2f b1) {
    asm("v_pk_mul_f32 %0, %2, %3 op_sel:[0,0] op_sel_hi:[1,0]\n\t"
        "v_pk_mul_f32 %1, %4, %5 op_sel:[0,0] op_sel_hi:[1,0]\n\t"
        "v_pk_fma_f32 %0, %2, %3, %0 op_sel:[1,1,0] op_sel_hi:[0,1,1] neg_lo:[1,0,0]\n\t"
        "v_pk_fma_f32 %1, %4, %5, %1 op_sel:[1,1,0] op_sel_hi:[0,1,1] neg_lo:[1,0,0]"
        : "=&v"(c0), "=&v"(c1) : "v"(a0), "v"(b0), "v"(a1), "v"(b1));
}

template <bool CONJ>
__device__ __forceinline__ v2f epow(float a2, float rh, float rl, float s) {
    float e = __builtin_amdgcn_exp2f(a2 * s);
    float x = __builtin_fmaf(rl, s, __builtin_amdgcn_fractf(rh * s));
    float cs = __builtin_amdgcn_cosf(x), sn = __builtin_amdgcn_sinf(x);
    v2f c;
    c[0] = e * cs;
    c[1] = CONJ ? -(e * sn) : (e * sn);
    return c;
}

#define H_COPY 4160
#define H_UNIT (4 * H_COPY)
#define R2_OFF (4 * H_UNIT)
#define TAB_PITCH 544
#define TAB_CH (64 * TAB_PITCH)
#define U_OFF R2_OFF
#define U_COPY 4640
#define U_UNIT (2 * U_COPY)
#define Y_OFF (U_OFF + 4 * U_UNIT)
#define T_OFF (R2_OFF + 2 * TAB_CH)
#define T_CH 5248
#define F_OFF (T_OFF + 2 * T_CH)
#define LDS_BYTES (F_OFF + 16)

#define WS_E 0
#define WS_INBOX 4096
#define WS_GATE (128 * 1024)
#define WS_UT (1024 * 1024)
#define WS_STAMPS (8 * 1024 * 1024)

#define SPIN_CAP (1u << 20)
#define RAW_BARRIER() do { asm volatile("s_waitcnt lgkmcnt(0)" ::: "memory"); __builtin_amdgcn_s_barrier(); asm volatile("" ::: "memory"); } while (0)

template <int CTRL>
__device__ __forceinline__ float dpp_add(float v) {
    int t = __builtin_amdgcn_update_dpp(0, __float_as_int(v), CTRL, 0xf, 0xf, true);
    return v + __int_as_float(t);
}

#define NSTEP 16
#define PASS_SPLIT 5
__host__ __device__ constexpr int pass_lo(int p) { return p == 0 ? 0 : PASS_SPLIT; }
__host__ __device__ constexpr int pass_hi(int p) { return p == 0 ? PASS_SPLIT - 1 : 7; }
__host__ __device__ constexpr int pass_nm(int p) {
    int c = 0;
    for (int i = 0; i <= pass_hi(p); i++) for (int q = 0; q <= pass_hi(p); q++) if (i + q >= pass_lo(p) && i + q <= pass_hi(p)) c++;
    return c;
}

template <int S>
__device__ __forceinline__ void load_set(v8bf (&A)[8], v8bf (&B)[8], lptr ha, lptr ub) {
    constexpr int P = S / 8, r = S % 8, N = pass_hi(P) + 1;
#pragma unroll
    for (int i = 0; i < N; i++) {
        A[i] = *(LAS v8bf*)(ha + (7 - r) * 64 + (7 - i) * 512);
        B[i] = *(LAS v8bf*)(ub + (7 - r) * 64 + i * 512);
    }
}
template <int S>
__device__ __forceinline__ void mma_set(const v8bf (&A)[8], const v8bf (&B)[8], v4f (&acc)[8]) {
    constexpr int P = S / 8, LO = pass_lo(P), HI = pass_hi(P);
#pragma unroll
    for (int i = 0; i <= HI; i++)
#pragma unroll
        for (int q = 0; q <= HI; q++)
            if (i + q >= LO && i + q <= HI)
                acc[i + q] = __builtin_amdgcn_mfma_f32_16x16x32_bf16(A[i], B[q], acc[i + q], 0, 0, 0);
}
template <int S>
__device__ __forceinline__ void sched_step() {
    constexpr int M = pass_nm(S / 8);
    constexpr int R = (S + 1 < NSTEP) ? 2 * (pass_hi((S + 1) / 8) + 1) : 0;
    constexpr int per = (R + M - 1) / M;
#pragma unroll
    for (int k = 0; k < M; k++) {
        __builtin_amdgcn_sched_group_barrier(0x8, 1, 0);
        if (R > 0) __builtin_amdgcn_sched_group_barrier(0x100, per, 0);
    }
}
template <int S>
__device__ __forceinline__ void conv_steps(v8bf (&A0)[8], v8bf (&B0)[8], v8bf (&A1)[8], v8bf (&B1)[8], v4f (&acc)[8], lptr ha, lptr ub, lptr yb, lptr flags, u64& amp_t0, u64& amp_t1) {
    if constexpr (S < NSTEP) {
        if constexpr (S + 1 < NSTEP) {
            if constexpr (S & 1) load_set<S + 1>(A0, B0, ha, ub); else load_set<S + 1>(A1, B1, ha, ub);
        }
        if constexpr (S & 1) mma_set<S>(A1, B1, acc); else mma_set<S>(A0, B0, acc);
        sched_step<S>();
        if constexpr (S % 8 == 7) {
            constexpr int P = S / 8;
#pragma unroll
            for (int t = pass_lo(P); t <= pass_hi(P); t++) {
                v2u o = {pk(acc[t][0], acc[t][1]), pk(acc[t][2], acc[t][3])};
                *(LAS v2u*)(yb + t * 512) = o;
            }
            asm volatile("s_waitcnt lgkmcnt(0)" ::: "memory");
            if ((threadIdx.x & 63) == 0) __hip_atomic_fetch_add((LAS u32*)(flags + P * 4), 1u, __ATOMIC_RELAXED, __HIP_MEMORY_SCOPE_WORKGROUP);
            STAMP(8 + P);
        }
        conv_steps<S + 1>(A0, B0, A1, B1, acc, ha, ub, yb, flags, amp_t0, amp_t1);
    }
}

__device__ __forceinline__ void wait_lds(lptr word, u32 want) {
    for (u32 spins = 0; spins < SPIN_CAP; spins++) {
        const u32 v = __hip_atomic_load((LAS u32*)word, __ATOMIC_RELAXED, __HIP_MEMORY_SCOPE_WORKGROUP);
        if (__builtin_amdgcn_readfirstlane(v) >= want) break;
        __builtin_amdgcn_s_sleep(2);
    }
    asm volatile("" ::: "memory");
}

__device__ __forceinline__ void epi_chunk(lptr sm, int T0, const u32 (&gv)[8], u32* __restrict__ out32, int p, float dA0, float dA1, float dS0, float dS1) {
    const v4u ya0 = *(LAS v4u*)(sm + Y_OFF + 0 * 4096 + T0 * 2), yb0 = *(LAS v4u*)(sm + Y_OFF + 1 * 4096 + T0 * 2);
    const v4u ya1 = *(LAS v4u*)(sm + Y_OFF + 2 * 4096 + T0 * 2), yb1 = *(LAS v4u*)(sm + Y_OFF + 3 * 4096 + T0 * 2);
    const lptr ub = sm + U_OFF + U_COPY + 512 + T0 * 2;
    const v4u kv0 = *(LAS v4u*)(ub + 0 * U_UNIT), x0 = *(LAS v4u*)(ub + 1 * U_UNIT);
    const v4u kv1 = *(LAS v4u*)(ub + 2 * U_UNIT), x1 = *(LAS v4u*)(ub + 3 * U_UNIT);
    u32* o = out32 + (u32)(T0 * 256 + p);
#pragma unroll
    for (int e = 0; e < 8; e++) {
        const int yd = (e & 1) * 2 + (e >> 2);
        const bool yh = (e >> 1) & 1;
        const int nd = e >> 1;
        const bool nh = e & 1;
        const float fya0 = yh ? bfhi(ya0[yd]) : bflo(ya0[yd]), fyb0 = yh ? bfhi(yb0[yd]) : bflo(yb0[yd]);
        const float fya1 = yh ? bfhi(ya1[yd]) : bflo(ya1[yd]), fyb1 = yh ? bfhi(yb1[yd]) : bflo(yb1[yd]);
        const float fkv0 = nh ? bfhi(kv0[nd]) : bflo(kv0[nd]), fx0 = nh ? bfhi(x0[nd]) : bflo(x0[nd]);
        const float fkv1 = nh ? bfhi(kv1[nd]) : bflo(kv1[nd]), fx1 = nh ? bfhi(x1[nd]) : bflo(x1[nd]);
        const float g = bflo(gv[e]), g1 = bfhi(gv[e]);
        const float a0 = fx0 * (fya0 + fkv0 * dA0), b0 = fyb0 + fx0 * dS0;
        const float a1 = fx1 * (fya1 + fkv1 * dA1), b1 = fyb1 + fx1 * dS1;
        o[e * 256] = pk(g * a0 + g1 * b0, g * a1 + g1 * b1);
    }
}

__global__ void __launch_bounds__(512, 2)
fused_kernel(const u16* __restrict__ X, const u16* __restrict__ Kin, const u16* __restrict__ V, const u16* __restrict__ shiftk,
             const u16* __restrict__ hA, const u16* __restrict__ ssmD, const u16* __restrict__ Are, const u16* __restrict__ Aim,
             const u16* __restrict__ Bre, const u16* __restrict__ Bim, const u16* __restrict__ Cre, const u16* __restrict__ Cim,
             const u16* __restrict__ logdt, const u16* __restrict__ Dskip, const u16* __restrict__ gatew,
             char* __restrict__ ws, u32* __restrict__ out32) {
    __shared__ __attribute__((aligned(16))) char smem_[LDS_BYTES];
    const lptr sm = (lptr)smem_;
    const int tid = threadIdx.x;
    const int bid = blockIdx.x;
    const int lane = tid & 63;
    const int w = __builtin_amdgcn_readfirstlane(tid >> 6);
    const int dg = bid & 7, tg = bid >> 3;
    const int p = dg * 32 + tg;
    u64 amp_t0 = 0, amp_t1 = 0;
    STAMP(0);

    const int cp = tid & 31, rb = tid >> 5;
    const int t0 = tg * 64 + rb * 4;
    const int c = dg * 64 + cp * 2;
    u32 kk[7], vv[4], xx[4];
    {
        const __amdgpu_buffer_rsrc_t k_rsrc = __builtin_amdgcn_make_buffer_rsrc((void*)Kin, 0, S_ * D_ * 2, 0x00020000);
        const u32 o = (u32)(t0 * D_ + c) * 2u;
        kk[0] = __builtin_amdgcn_raw_buffer_load_b32(k_rsrc, o - 3072u, 0, 0);
        kk[1] = __builtin_amdgcn_raw_buffer_load_b32(k_rsrc, o - 2048u, 0, 0);
        kk[2] = __builtin_amdgcn_raw_buffer_load_b32(k_rsrc, o - 1024u, 0, 0);
#pragma unroll
        for (int j = 0; j < 4; j++) kk[3 + j] = ldg<u32>(Kin, o + j * 1024);
#pragma unroll
        for (int j = 0; j < 4; j++) vv[j] = ldg<u32>(V, o + j * 1024);
#pragma unroll
        for (int j = 0; j < 4; j++) xx[j] = ldg<u32>(X, o + j * 1024);
    }
    const v4u sk = ldg<v4u>(shiftk, (u32)c * 8u);
    const int kch = (tid >> 7) & 1, kn = tid & 127;
    const int kd = p * 2 + kch;
    const u32 pi = (u32)(kd * N_ + kn) * 2u;
    const u16 p_ldt = ldg<u16>(logdt, (u32)kd * 2u);
    const u16 p_are = ldg<u16>(Are, pi), p_aim = ldg<u16>(Aim, pi), p_bre = ldg<u16>(Bre, pi), p_bim = ldg<u16>(Bim, pi), p_cre = ldg<u16>(Cre, pi), p_cim = ldg<u16>(Cim, pi);
    const u64 E = __hip_atomic_load((GAS u64*)(ws + WS_E), __ATOMIC_RELAXED, __HIP_MEMORY_SCOPE_AGENT);
    const int hch = tid >> 8, hck = tid & 255;
    const u32 ho = (u32)((p * 2 + hch) * S_ + hck * 8) * 2u;
    const v4u hd = ldg<v4u>(hA, ho);
    v4u hm = ldg<v4u>(hA, ho - (hck ? 16u : 0u));
    const int tgate = bid * 8 + w;
    const v4u gx = ldg<v4u>(X, (u32)(tgate * D_ + lane * 8) * 2u);
    const v4u gw = ldg<v4u>(gatew, (u32)lane * 16u);
    const u32 dAp = *(const u32*)(ssmD + p * 2), dSp = *(const u32*)(Dskip + p * 2);
    __builtin_amdgcn_sched_barrier(0);
    STAMP(13);

    if (tid < 4) *(LAS u32*)(sm + F_OFF + tid * 4) = 0;
    {
        float s0[4] = {bflo(sk[0]), bfhi(sk[0]), bflo(sk[1]), bfhi(sk[1])};
        float s1[4] = {bflo(sk[2]), bfhi(sk[2]), bflo(sk[3]), bfhi(sk[3])};
        u32 kvp[4];
#pragma unroll
        for (int j = 0; j < 4; j++) {
            float a0 = bflo(kk[j]) * s0[3] + bflo(kk[j + 1]) * s0[2] + bflo(kk[j + 2]) * s0[1] + bflo(kk[j + 3]) * s0[0];
            float a1 = bfhi(kk[j]) * s1[3] + bfhi(kk[j + 1]) * s1[2] + bfhi(kk[j + 2]) * s1[1] + bfhi(kk[j + 3]) * s1[0];
            kvp[j] = pk(a0 * bflo(vv[j]), a1 * bfhi(vv[j]));
        }
        STAMP(1);
        const int sw = (rb ^ ((cp & 7) << 1)) * 8;
        const lptr base = sm + (cp * 2) * 128 + sw;
        v2u a, b;
        a[0] = __builtin_amdgcn_perm(kvp[1], kvp[0], 0x05040100); a[1] = __builtin_amdgcn_perm(kvp[3], kvp[2], 0x05040100);
        b[0] = __builtin_amdgcn_perm(kvp[1], kvp[0], 0x07060302); b[1] = __builtin_amdgcn_perm(kvp[3], kvp[2], 0x07060302);
        *(LAS v2u*)(base) = a; *(LAS v2u*)(base + 128) = b;
        a[0] = __builtin_amdgcn_perm(xx[1], xx[0], 0x05040100); a[1] = __builtin_amdgcn_perm(xx[3], xx[2], 0x05040100);
        b[0] = __builtin_amdgcn_perm(xx[1], xx[0], 0x07060302); b[1] = __builtin_amdgcn_perm(xx[3], xx[2], 0x07060302);
        *(LAS v2u*)(base + 8192) = a; *(LAS v2u*)(base + 8192 + 128) = b;
    }
    RAW_BARRIER();
    const __amdgpu_buffer_rsrc_t ut_rsrc = __builtin_amdgcn_make_buffer_rsrc(ws + WS_UT, 0, 2 * D_ * S_ * 2, 0x00020000);
    {
#pragma unroll
        for (int i = 0; i < 2; i++) {
            const int ch = (tid >> 3) & 63, ck = tid & 7;
            const int sw = (ck ^ ((ch >> 1) & 7)) * 16;
            v4u d = *(LAS v4u*)(sm + i * 8192 + ch * 128 + sw);
            __builtin_amdgcn_raw_buffer_store_b128(d, ut_rsrc, (u32)(((i * D_ + dg * 64 + ch) * S_ + tg * 64 + ck * 8) * 2), 0, 16);
        }
    }
    STAMP(2);

    {
        const float dt = rbf(expf(bf1(p_ldt)));
        const float a = dt * bf1(p_are), b = dt * bf1(p_aim);
        const float br = dt * bf1(p_bre), bi = dt * bf1(p_bim);
        const float cre = bf1(p_cre), cim = bf1(p_cim);
        v2f CB;
        CB[0] = cre * br - cim * bi;
        CB[1] = -(cre * bi + cim * br);
        const float a2 = a * 1.4426950408889634f;
        const float rh = b * 0.15915494309189535f;
        const float rl = __builtin_fmaf(b, 0.15915494309189535f, -rh) + b * 6.4206383e-9f;
        const lptr tab = sm + R2_OFF + kch * TAB_CH + kn * 4;
#define PUT(row, v) (*(LAS u32*)(tab + (row) * TAB_PITCH) = pk2(v))
        if (w < 4) {
            v2f E0 = epow<true>(a2, rh, rl, 1.f), E1 = epow<true>(a2, rh, rl, 2.f), E2 = epow<true>(a2, rh, rl, 4.f), E7 = epow<true>(a2, rh, rl, 128.f);
            v2f F1 = epow<false>(a2, rh, rl, 8.f), F2 = epow<false>(a2, rh, rl, 16.f), F4 = epow<false>(a2, rh, rl, 32.f), F8 = epow<false>(a2, rh, rl, 64.f);
            v2f P[8], F[8], t0_, t1_;
            P[0] = CB;
            cmul2(P[1], P[0], E0, P[2], P[0], E1);
            cmul2(P[3], P[1], E1, P[4], P[0], E2);
            cmul2(P[5], P[1], E2, P[6], P[2], E2);
            cmul2(P[7], P[3], E2, F[3], F1, F2);
            F[0][0] = 1.f; F[0][1] = 0.f; F[1] = F1; F[2] = F2; F[4] = F4;
            cmul2(F[5], F4, F1, F[6], F4, F2);
            cmul2(F[7], F4, F[3], t0_, F8, F1);
            PUT(48 + 9, t0_);
#pragma unroll
            for (int e = 0; e < 8; e += 2) {
                PUT(e, P[e]); PUT(e + 1, P[e + 1]);
                cmul2(t0_, P[e], E7, t1_, P[e + 1], E7);
                PUT(8 + e, t0_); PUT(9 + e, t1_);
            }
#pragma unroll
            for (int e = 0; e < 8; e++) PUT(48 + e, F[e]);
            PUT(48 + 8, F8);
#pragma unroll
            for (int e = 2; e < 8; e += 2) {
                cmul2(t0_, F8, F[e], t1_, F8, F[e + 1]);
                PUT(48 + 8 + e, t0_); PUT(48 + 9 + e, t1_);
            }
        } else {
            v2f E1 = epow<true>(a2, rh, rl, 2.f), E2 = epow<true>(a2, rh, rl, 4.f), E7 = epow<true>(a2, rh, rl, 128.f), E8 = epow<true>(a2, rh, rl, 256.f);
            v2f E9 = epow<true>(a2, rh, rl, 512.f), E10 = epow<true>(a2, rh, rl, 1024.f), E11 = epow<true>(a2, rh, rl, 2048.f);
            v2f P2, P4, P6, W3, R[4], Q0, Q1, T0, T1, T2, T3, t0_, t1_;
            cmul2(P2, CB, E1, P4, CB, E2);
            cmul2(P6, P2, E2, W3, E7, E8);
            cmul2(R[0], CB, E8, R[1], P2, E8);
            cmul2(R[2], P4, E8, R[3], P6, E8);
#pragma unroll
            for (int e = 0; e < 4; e += 2) {
                PUT(16 + e, R[e]); PUT(17 + e, R[e + 1]);
                cmul2(t0_, R[e], E7, t1_, R[e + 1], E7);
                PUT(20 + e, t0_); PUT(21 + e, t1_);
            }
            cmul2(Q0, CB, E9, Q1, P4, E9);
            PUT(24, Q0); PUT(28, Q1);
            cmul2(t0_, Q0, E7, t1_, Q1, E7); PUT(25, t0_); PUT(29, t1_);
            cmul2(t0_, Q0, E8, t1_, Q1, E8); PUT(26, t0_); PUT(30, t1_);
            cmul2(t0_, Q0, W3, t1_, Q1, W3); PUT(27, t0_); PUT(31, t1_);
            cmul2(T0, CB, E10, t0_, CB, E11); PUT(32, T0); PUT(40, t0_);
            cmul2(T1, T0, E7, T2, T0, E8); PUT(33, T1); PUT(34, T2);
            cmul2(T3, T0, W3, t0_, T0, E9); PUT(35, T3); PUT(36, t0_);
            cmul2(t0_, T1, E9, t1_, T2, E9); PUT(37, t0_); PUT(38, t1_);
            cmul2(t0_, T3, E9, t1_, T3, E9); PUT(39, t0_);
        }
    }
    STAMP(3);
    const u64 epoch = (u64)__builtin_amdgcn_readfirstlane((u32)E) + ((u64)__builtin_amdgcn_readfirstlane((u32)(E >> 32)) << 32) + 1ull;
    asm volatile("s_waitcnt vmcnt(0)" ::: "memory");
    RAW_BARRIER();
    STAMP(4);
    if (w == 0 && lane < 32) {
        GAS u64* inbox = (GAS u64*)(ws + WS_INBOX) + (dg * 32 + lane) * 32 + tg;
        __hip_atomic_store(inbox, epoch, __ATOMIC_RELAXED, __HIP_MEMORY_SCOPE_AGENT);
    }
    {
        float s = 0.f;
#pragma unroll
        for (int j = 0; j < 4; j++) {
            u32 pp = pk(bflo(gx[j]) * bflo(gw[j]), bfhi(gx[j]) * bfhi(gw[j]));
            s += bflo(pp) + bfhi(pp);
        }
        s = dpp_add<0x111>(s);
        s = dpp_add<0x112>(s);
        s = dpp_add<0x114>(s);
        s = dpp_add<0x118>(s);
        const int si = __float_as_int(s);
        float tot = __int_as_float(__builtin_amdgcn_readlane(si, 15)) + __int_as_float(__builtin_amdgcn_readlane(si, 31)) +
                    __int_as_float(__builtin_amdgcn_readlane(si, 47)) + __int_as_float(__builtin_amdgcn_readlane(si, 63));
        float z = rbf(tot);
        float g = 1.f / (1.f + __expf(-z));
        u32 gb = pk(g, g) & 0xffff;
        if (lane == 0) __hip_atomic_store((GAS u64*)(ws + WS_GATE) + tgate, (epoch << 16) | gb, __ATOMIC_RELAXED, __HIP_MEMORY_SCOPE_AGENT);
    }
    if (w < 6) {
        const int c2 = (w >= 3) ? 1 : 0, mt = w - c2 * 3;
        const lptr ga = sm + R2_OFF + c2 * TAB_CH + (mt * 16 + (lane & 15)) * TAB_PITCH + (lane >> 4) * 16;
        const lptr fb = sm + R2_OFF + c2 * TAB_CH + (48 + (lane & 15)) * TAB_PITCH + (lane >> 4) * 16;
        v4f acc = {0.f, 0.f, 0.f, 0.f};
#pragma unroll
        for (int s = 0; s < 8; s++) {
            v8bf A = *(LAS v8bf*)(ga + s * 64);
            v8bf B = *(LAS v8bf*)(fb + s * 64);
            acc = __builtin_amdgcn_mfma_f32_16x16x32_bf16(A, B, acc, 0, 0, 0);
        }
        const int g4 = lane >> 4;
        int base, step;
        if (mt == 0) { base = ((g4 >> 1) << 7) + ((g4 & 1) << 2); step = 1; }
        else if (mt == 1) { base = (g4 < 2) ? (256 + (g4 << 7)) : (512 + ((g4 & 1) << 2)); step = (g4 < 2) ? 2 : 128; }
        else { base = (g4 < 2) ? (1024 + (g4 << 9)) : (2048 + ((g4 & 1) << 3)); step = 128; }
        const lptr T = sm + T_OFF + c2 * T_CH + (base + 8 * (lane & 15)) * 2;
        const u32 o01 = pk(acc[0], acc[1]), o23 = pk(acc[2], acc[3]);
        *(LAS u16*)(T) = (u16)o01;
        *(LAS u16*)(T + step * 2) = (u16)(o01 >> 16);
        *(LAS u16*)(T + step * 4) = (u16)o23;
        *(LAS u16*)(T + step * 6) = (u16)(o23 >> 16);
    }
    {
        if (hck == 0) { hm[1] = 0; hm[2] = 0; hm[3] = 0; }
        u32 r[7];
        r[0] = rot16(hd[3]); r[1] = rot16(hd[2]); r[2] = rot16(hd[1]); r[3] = rot16(hd[0]);
        r[4] = rot16(hm[3]); r[5] = rot16(hm[2]); r[6] = rot16(hm[1]);
        const lptr dst = sm + (hch * 2) * H_UNIT + (255 - hck) * 16;
        *(LAS v4u*)(dst) = (v4u){r[3], r[4], r[5], r[6]};
        *(LAS v4u*)(dst + H_COPY) = (v4u){r[2], r[3], r[4], r[5]};
        *(LAS v4u*)(dst + 2 * H_COPY) = (v4u){r[1], r[2], r[3], r[4]};
        *(LAS v4u*)(dst + 3 * H_COPY) = (v4u){r[0], r[1], r[2], r[3]};
        if (tid < 48) {
            const int uc = tid / 3, ck = tid - uc * 3;
            *(LAS v4u*)(sm + uc * H_COPY + (256 + ck) * 16) = (v4u){0, 0, 0, 0};
        }
    }
    RAW_BARRIER();
    STAMP(5);
    {
        const lptr T = sm + T_OFF + hch * T_CH;
        const int l0 = hck * 8;
        u32 hv[14];
#pragma unroll
        for (int j = 0; j < 14; j++) {
            const int l = l0 - 6 + j;
            const u32 lv = (u32)rbf((float)(j < 6 ? max(l, 0) : l));
            const u32 x = *(LAS u16*)(T + lv * 2);
            hv[j] = (j < 6 && l < 0) ? 0u : x;
        }
        u32 r[7];
#pragma unroll
        for (int i = 0; i < 7; i++) r[i] = hv[13 - 2 * i] | (hv[12 - 2 * i] << 16);
        const lptr dst = sm + (hch * 2 + 1) * H_UNIT + (255 - hck) * 16;
        *(LAS v4u*)(dst) = (v4u){r[3], r[4], r[5], r[6]};
        *(LAS v4u*)(dst + H_COPY) = (v4u){r[2], r[3], r[4], r[5]};
        *(LAS v4u*)(dst + 2 * H_COPY) = (v4u){r[1], r[2], r[3], r[4]};
        *(LAS v4u*)(dst + 3 * H_COPY) = (v4u){r[0], r[1], r[2], r[3]};
        if (tid < 256) *(LAS v4u*)(sm + U_OFF + (tid >> 5) * U_COPY + (tid & 31) * 16) = (v4u){0, 0, 0, 0};
    }
    STAMP(6);
    {
        const int pidx = w * 4 - 1 + min(lane, 4);
        GAS u64* inbox = (GAS u64*)(ws + WS_INBOX) + p * 32 + max(pidx, 0);
        for (u32 spins = 0; spins < SPIN_CAP; spins++) {
            const u64 v = __hip_atomic_load(inbox, __ATOMIC_RELAXED, __HIP_MEMORY_SCOPE_AGENT);
            if (__all(v == epoch)) break;
            __builtin_amdgcn_s_sleep(1);
        }
        asm volatile("" ::: "memory");
        STAMP(7);
        v4u ud[2];
        u32 up[2];
#pragma unroll
        for (int i = 0; i < 2; i++) {
            const int u = i * 2 + (lane >> 5);
            const int t = w * 256 + (lane & 31) * 8;
            const u32 off = (u32)((((u & 1) * D_ + p * 2 + (u >> 1)) * S_ + t) * 2);
            ud[i] = __builtin_amdgcn_raw_buffer_load_b128(ut_rsrc, off, 0, 16);
            up[i] = __builtin_amdgcn_raw_buffer_load_b32(ut_rsrc, t ? off - 4u : off, 0, 16);
        }
#pragma unroll
        for (int i = 0; i < 2; i++) {
            const int u = i * 2 + (lane >> 5);
            const int t = w * 256 + (lane & 31) * 8;
            const lptr dst = sm + U_OFF + u * U_UNIT + 512 + t * 2;
            const u32 prev = t ? up[i] : 0u;
            *(LAS v4u*)(dst + U_COPY) = ud[i];
            *(LAS v4u*)(dst) = (v4u){__builtin_amdgcn_alignbit(ud[i][0], prev, 16), __builtin_amdgcn_alignbit(ud[i][1], ud[i][0], 16),
                                     __builtin_amdgcn_alignbit(ud[i][2], ud[i][1], 16), __builtin_amdgcn_alignbit(ud[i][3], ud[i][2], 16)};
        }
    }
    float dA0 = bflo(dAp), dA1 = bfhi(dAp), dS0 = bflo(dSp), dS1 = bfhi(dSp);
    asm volatile("" : "+v"(dA0), "+v"(dA1), "+v"(dS0), "+v"(dS1));
    RAW_BARRIER();
    STAMP(11);
    if (w < 4) {
        const int g4 = lane >> 4, n = lane & 15;
        const lptr ha = sm + w * H_UNIT + (n & 3) * H_COPY + 48 - 16 * (n >> 2) + 16 * g4;
        const lptr ub = sm + U_OFF + w * U_UNIT + (n & 1) * U_COPY + 512 + 64 * (n >> 1) + 16 * g4 - 7 * 64;
        const lptr yb = sm + Y_OFF + w * 4096 + 64 * (n >> 1) + 16 * g4 + 8 * (n & 1);
        v8bf A0[8], B0[8], A1[8], B1[8];
        v4f acc[8];
#pragma unroll
        for (int i = 0; i < 8; i++) acc[i] = (v4f){0.f, 0.f, 0.f, 0.f};
        load_set<0>(A0, B0, ha, ub);
        conv_steps<0>(A0, B0, A1, B1, acc, ha, ub, yb, sm + F_OFF, amp_t0, amp_t1);
        if (bid == 0 && w == 0) {
            const u64 now = __builtin_amdgcn_s_memrealtime();
            const u64 until = now + 4 * (amp_t1 - amp_t0);
            while (__builtin_amdgcn_s_memrealtime() < until) __builtin_amdgcn_s_sleep(1);
        }
    } else {
        const int ck = tid - 256;
        u32 gv[8];
        {
            GAS u64* gr = (GAS u64*)(ws + WS_GATE) + ck * 8;
            const u64 tag = epoch & 0xFFFFFFFFFFFFull;
            for (u32 spins = 0; spins < SPIN_CAP; spins++) {
                bool ok = true;
#pragma unroll
                for (int i = 0; i < 8; i++) {
                    const u64 v = __hip_atomic_load(gr + i, __ATOMIC_RELAXED, __HIP_MEMORY_SCOPE_AGENT);
                    gv[i] = (u32)v & 0xffff;
                    ok &= (v >> 16) == tag;
                }
                if (__all(ok)) break;
                __builtin_amdgcn_s_sleep(1);
            }
#pragma unroll
            for (int i = 0; i < 8; i++) gv[i] |= pk(0.f, 1.f - bf1((u16)gv[i])) & 0xffff0000u;
        }
        if (bid == 0 && lane == 0) {
            const u32 c4 = __hip_atomic_fetch_add((LAS u32*)(sm + F_OFF + 8), 1u, __ATOMIC_RELAXED, __HIP_MEMORY_SCOPE_WORKGROUP);
            if (c4 == 3) __hip_atomic_store((GAS u64*)(ws + WS_E), epoch, __ATOMIC_RELAXED, __HIP_MEMORY_SCOPE_AGENT);
        }
        STAMP(8);
        if (w < 7) {
            wait_lds(sm + F_OFF, 4);
            STAMP(9);
            if (ck < PASS_SPLIT * 32) epi_chunk(sm, ck * 8, gv, out32, p, dA0, dA1, dS0, dS1);
        }
        if (w >= 6) {
            wait_lds(sm + F_OFF + 4, 4);
            STAMP(10);
            if (ck >= PASS_SPLIT * 32) epi_chunk(sm, ck * 8, gv, out32, p, dA0, dA1, dS0, dS1);
        }
        STAMP(12);
    }
}

extern "C" void kernel_launch(void* const* d_in, const int* in_sizes, int n_in,
                              void* d_out, int out_size, void* d_ws, size_t ws_size,
                              hipStream_t stream) {
    fused_kernel<<<256, 512, 0, stream>>>((const u16*)d_in[0], (const u16*)d_in[1], (const u16*)d_in[2], (const u16*)d_in[3],
                                          (const u16*)d_in[4], (const u16*)d_in[5], (const u16*)d_in[7], (const u16*)d_in[8],
                                          (const u16*)d_in[9], (const u16*)d_in[10], (const u16*)d_in[11], (const u16*)d_in[12],
                                          (const u16*)d_in[13], (const u16*)d_in[14], (const u16*)d_in[15], (char*)d_ws, (u32*)d_out);
}
```

```cpp
#include <hip/hip_runtime.h>
#include <stdint.h>

typedef unsigned short u16;
typedef unsigned int u32;
typedef unsigned long long u64;
typedef short v4s __attribute__((ext_vector_type(4)));
typedef short v8s __attribute__((ext_vector_type(8)));
typedef __bf16 v8bf __attribute__((ext_vector_type(8)));
typedef __bf16 v2bf __attribute__((ext_vector_type(2)));
typedef float v4f __attribute__((ext_vector_type(4)));
typedef float v2f __attribute__((ext_vector_type(2)));
typedef u32 v4u __attribute__((ext_vector_type(4)));
typedef u32 v2u __attribute__((ext_vector_type(2)));
#define LAS __attribute__((address_space(3)))
#define GAS __attribute__((address_space(1)))
typedef LAS char* lptr;

#define S_ 2048
#define D_ 512
#define N_ 128

#define STAMP(i) do { } while (0)

__device__ __forceinline__ float bflo(u32 v) { return __uint_as_float(v << 16); }
__device__ __forceinline__ float bfhi(u32 v) { return __uint_as_float(v & 0xffff0000u); }
__device__ __forceinline__ float bf1(u16 v) { return __uint_as_float(((u32)v) << 16); }
__device__ __forceinline__ u32 pk(float lo, float hi) {
    v2f x = {lo, hi};
    return __builtin_bit_cast(u32, __builtin_convertvector(x, v2bf));
}
__device__ __forceinline__ u32 pk2(v2f x) { return __builtin_bit_cast(u32, __builtin_convertvector(x, v2bf)); }
__device__ __forceinline__ u32 rot16(u32 v) { return __builtin_amdgcn_alignbit(v, v, 16); }
__device__ __forceinline__ float rbf(float f) { return bflo(pk(f, f)); }

template <typename T>
__device__ __forceinline__ T ldg(const void* base, u32 off) { return *(const GAS T*)((const GAS char*)base + off); }

__device__ __forceinline__ void cmul2(v2f& c0, v2f a0, v2f b0, v2f& c1, v2f a1, v2f b1) {
    asm("v_pk_mul_f32 %0, %2, %3 op_sel:[0,0] op_sel_hi:[1,0]\n\t"
        "v_pk_mul_f32 %1, %4, %5 op_sel:[0,0] op_sel_hi:[1,0]\n\t"
        "v_pk_fma_f32 %0, %2, %3, %0 op_sel:[1,1,0] op_sel_hi:[0,1,1] neg_lo:[1,0,0]\n\t"
        "v_pk_fma_f32 %1, %4, %5, %1 op_sel:[1,1,0] op_sel_hi:[0,1,1] neg_lo:[1,0,0]"
        : "=&v"(c0), "=&v"(c1) : "v"(a0), "v"(b0), "v"(a1), "v"(b1));
}

template <bool CONJ>
__device__ __forceinline__ v2f epow(float a2, float rh, float rl, float s) {
    float e = __builtin_amdgcn_exp2f(a2 * s);
    float x = __builtin_fmaf(rl, s, __builtin_amdgcn_fractf(rh * s));
    float cs = __builtin_amdgcn_cosf(x), sn = __builtin_amdgcn_sinf(x);
    v2f c;
    c[0] = e * cs;
    c[1] = CONJ ? -(e * sn) : (e * sn);
    return c;
}

#define H_COPY 4160
#define H_UNIT (4 * H_COPY)
#define R2_OFF (4 * H_UNIT)
#define TAB_PITCH 544
#define TAB_CH (64 * TAB_PITCH)
#define U_OFF R2_OFF
#define U_COPY 4640
#define U_UNIT (2 * U_COPY)
#define Y_OFF (U_OFF + 4 * U_UNIT)
#define T_OFF (R2_OFF + 2 * TAB_CH)
#define T_CH 5248
#define F_OFF (T_OFF + 2 * T_CH)
#define LDS_BYTES (F_OFF + 16)

#define WS_E 0
#define WS_INBOX 4096
#define WS_GATE (128 * 1024)
#define WS_UT (1024 * 1024)
#define WS_STAMPS (8 * 1024 * 1024)

#define SPIN_CAP (1u << 20)
#define RAW_BARRIER() do { asm volatile("s_waitcnt lgkmcnt(0)" ::: "memory"); __builtin_amdgcn_s_barrier(); asm volatile("" ::: "memory"); } while (0)

template <int CTRL>
__device__ __forceinline__ float dpp_add(float v) {
    int t = __builtin_amdgcn_update_dpp(0, __float_as_int(v), CTRL, 0xf, 0xf, true);
    return v + __int_as_float(t);
}

#define NSTEP 16
#define PASS_SPLIT 5
__host__ __device__ constexpr int pass_lo(int p) { return p == 0 ? 0 : PASS_SPLIT; }
__host__ __device__ constexpr int pass_hi(int p) { return p == 0 ? PASS_SPLIT - 1 : 7; }
__host__ __device__ constexpr int pass_nm(int p) {
    int c = 0;
    for (int i = 0; i <= pass_hi(p); i++) for (int q = 0; q <= pass_hi(p); q++) if (i + q >= pass_lo(p) && i + q <= pass_hi(p)) c++;
    return c;
}

template <int S>
__device__ __forceinline__ void load_set(v8bf (&A)[8], v8bf (&B)[8], lptr ha, lptr ub) {
    constexpr int P = S / 8, r = S % 8, N = pass_hi(P) + 1;
#pragma unroll
    for (int i = 0; i < N; i++) {
        A[i] = *(LAS v8bf*)(ha + (7 - r) * 64 + (7 - i) * 512);
        B[i] = *(LAS v8bf*)(ub + (7 - r) * 64 + i * 512);
    }
}
template <int S>
__device__ __forceinline__ void mma_set(const v8bf (&A)[8], const v8bf (&B)[8], v4f (&acc)[8]) {
    constexpr int P = S / 8, LO = pass_lo(P), HI = pass_hi(P);
#pragma unroll
    for (int i = 0; i <= HI; i++)
#pragma unroll
        for (int q = 0; q <= HI; q++)
            if (i + q >= LO && i + q <= HI)
                acc[i + q] = __builtin_amdgcn_mfma_f32_16x16x32_bf16(A[i], B[q], acc[i + q], 0, 0, 0);
}
template <int S>
__device__ __forceinline__ void sched_step() {
    constexpr int M = pass_nm(S / 8);
    constexpr int R = (S + 1 < NSTEP) ? 2 * (pass_hi((S + 1) / 8) + 1) : 0;
    constexpr int per = (R + M - 1) / M;
#pragma unroll
    for (int k = 0; k < M; k++) {
        __builtin_amdgcn_sched_group_barrier(0x8, 1, 0);
        if (R > 0) __builtin_amdgcn_sched_group_barrier(0x100, per, 0);
    }
}
template <int S>
__device__ __forceinline__ void conv_steps(v8bf (&A0)[8], v8bf (&B0)[8], v8bf (&A1)[8], v8bf (&B1)[8], v4f (&acc)[8], lptr ha, lptr ub, lptr yb, lptr flags
) {
    if constexpr (S < NSTEP) {
        if constexpr (S + 1 < NSTEP) {
            if constexpr (S & 1) load_set<S + 1>(A0, B0, ha, ub); else load_set<S + 1>(A1, B1, ha, ub);
        }
        if constexpr (S & 1) mma_set<S>(A1, B1, acc); else mma_set<S>(A0, B0, acc);
        sched_step<S>();
        if constexpr (S % 8 == 7) {
            constexpr int P = S / 8;
#pragma unroll
            for (int t = pass_lo(P); t <= pass_hi(P); t++) {
                v2u o = {pk(acc[t][0], acc[t][1]), pk(acc[t][2], acc[t][3])};
                *(LAS v2u*)(yb + t * 512) = o;
            }
            asm volatile("s_waitcnt lgkmcnt(0)" ::: "memory");
            if ((threadIdx.x & 63) == 0) __hip_atomic_fetch_add((LAS u32*)(flags + P * 4), 1u, __ATOMIC_RELAXED, __HIP_MEMORY_SCOPE_WORKGROUP);
            STAMP(8 + P);
        }
        conv_steps<S + 1>(A0, B0, A1, B1, acc, ha, ub, yb, flags
        );
    }
}

__device__ __forceinline__ void wait_lds(lptr word, u32 want) {
    for (u32 spins = 0; spins < SPIN_CAP; spins++) {
        const u32 v = __hip_atomic_load((LAS u32*)word, __ATOMIC_RELAXED, __HIP_MEMORY_SCOPE_WORKGROUP);
        if (__builtin_amdgcn_readfirstlane(v) >= want) break;
        __builtin_amdgcn_s_sleep(2);
    }
    asm volatile("" ::: "memory");
}

__device__ __forceinline__ void epi_chunk(lptr sm, int T0, const u32 (&gv)[8], u32* __restrict__ out32, int p, float dA0, float dA1, float dS0, float dS1) {
    const v4u ya0 = *(LAS v4u*)(sm + Y_OFF + 0 * 4096 + T0 * 2), yb0 = *(LAS v4u*)(sm + Y_OFF + 1 * 4096 + T0 * 2);
    const v4u ya1 = *(LAS v4u*)(sm + Y_OFF + 2 * 4096 + T0 * 2), yb1 = *(LAS v4u*)(sm + Y_OFF + 3 * 4096 + T0 * 2);
    const lptr ub = sm + U_OFF + U_COPY + 512 + T0 * 2;
    const v4u kv0 = *(LAS v4u*)(ub + 0 * U_UNIT), x0 = *(LAS v4u*)(ub + 1 * U_UNIT);
    const v4u kv1 = *(LAS v4u*)(ub + 2 * U_UNIT), x1 = *(LAS v4u*)(ub + 3 * U_UNIT);
    u32* o = out32 + (u32)(T0 * 256 + p);
#pragma unroll
    for (int e = 0; e < 8; e++) {
        const int yd = (e & 1) * 2 + (e >> 2);
        const bool yh = (e >> 1) & 1;
        const int nd = e >> 1;
        const bool nh = e & 1;
        const float fya0 = yh ? bfhi(ya0[yd]) : bflo(ya0[yd]), fyb0 = yh ? bfhi(yb0[yd]) : bflo(yb0[yd]);
        const float fya1 = yh ? bfhi(ya1[yd]) : bflo(ya1[yd]), fyb1 = yh ? bfhi(yb1[yd]) : bflo(yb1[yd]);
        const float fkv0 = nh ? bfhi(kv0[nd]) : bflo(kv0[nd]), fx0 = nh ? bfhi(x0[nd]) : bflo(x0[nd]);
        const float fkv1 = nh ? bfhi(kv1[nd]) : bflo(kv1[nd]), fx1 = nh ? bfhi(x1[nd]) : bflo(x1[nd]);
        const float g = bflo(gv[e]), g1 = bfhi(gv[e]);
        const float a0 = fx0 * (fya0 + fkv0 * dA0), b0 = fyb0 + fx0 * dS0;
        const float a1 = fx1 * (fya1 + fkv1 * dA1), b1 = fyb1 + fx1 * dS1;
        o[e * 256] = pk(g * a0 + g1 * b0, g * a1 + g1 * b1);
    }
}

__global__ void __launch_bounds__(512, 2)
fused_kernel(const u16* __restrict__ X, const u16* __restrict__ Kin, const u16* __restrict__ V, const u16* __restrict__ shiftk,
             const u16* __restrict__ hA, const u16* __restrict__ ssmD, const u16* __restrict__ Are, const u16* __restrict__ Aim,
             const u16* __restrict__ Bre, const u16* __restrict__ Bim, const u16* __restrict__ Cre, const u16* __restrict__ Cim,
             const u16* __restrict__ logdt, const u16* __restrict__ Dskip, const u16* __restrict__ gatew,
             char* __restrict__ ws, u32* __restrict__ out32) {
    __shared__ __attribute__((aligned(16))) char smem_[LDS_BYTES];
    const lptr sm = (lptr)smem_;
    const int tid = threadIdx.x;
    const int bid = blockIdx.x;
    const int lane = tid & 63;
    const int w = __builtin_amdgcn_readfirstlane(tid >> 6);
    const int dg = bid & 7, tg = bid >> 3;
    const int p = dg * 32 + tg;
    STAMP(0);

    const int cp = tid & 31, rb = tid >> 5;
    const int t0 = tg * 64 + rb * 4;
    const int c = dg * 64 + cp * 2;
    u32 kk[7], vv[4], xx[4];
    {
        const __amdgpu_buffer_rsrc_t k_rsrc = __builtin_amdgcn_make_buffer_rsrc((void*)Kin, 0, S_ * D_ * 2, 0x00020000);
        const u32 o = (u32)(t0 * D_ + c) * 2u;
        kk[0] = __builtin_amdgcn_raw_buffer_load_b32(k_rsrc, o - 3072u, 0, 0);
        kk[1] = __builtin_amdgcn_raw_buffer_load_b32(k_rsrc, o - 2048u, 0, 0);
        kk[2] = __builtin_amdgcn_raw_buffer_load_b32(k_rsrc, o - 1024u, 0, 0);
#pragma unroll
        for (int j = 0; j < 4; j++) kk[3 + j] = ldg<u32>(Kin, o + j * 1024);
#pragma unroll
        for (int j = 0; j < 4; j++) vv[j] = ldg<u32>(V, o + j * 1024);
#pragma unroll
        for (int j = 0; j < 4; j++) xx[j] = ldg<u32>(X, o + j * 1024);
    }
    const v4u sk = ldg<v4u>(shiftk, (u32)c * 8u);
    const int kch = (tid >> 7) & 1, kn = tid & 127;
    const int kd = p * 2 + kch;
    const u32 pi = (u32)(kd * N_ + kn) * 2u;
    const u16 p_ldt = ldg<u16>(logdt, (u32)kd * 2u);
    const u16 p_are = ldg<u16>(Are, pi), p_aim = ldg<u16>(Aim, pi), p_bre = ldg<u16>(Bre, pi), p_bim = ldg<u16>(Bim, pi), p_cre = ldg<u16>(Cre, pi), p_cim = ldg<u16>(Cim, pi);
    const u64 E = __hip_atomic_load((GAS u64*)(ws + WS_E), __ATOMIC_RELAXED, __HIP_MEMORY_SCOPE_AGENT);
    const int hch = tid >> 8, hck = tid & 255;
    const u32 ho = (u32)((p * 2 + hch) * S_ + hck * 8) * 2u;
    const v4u hd = ldg<v4u>(hA, ho);
    v4u hm = ldg<v4u>(hA, ho - (hck ? 16u : 0u));
    const int tgate = bid * 8 + w;
    const v4u gx = ldg<v4u>(X, (u32)(tgate * D_ + lane * 8) * 2u);
    const v4u gw = ldg<v4u>(gatew, (u32)lane * 16u);
    const u32 dAp = *(const u32*)(ssmD + p * 2), dSp = *(const u32*)(Dskip + p * 2);
    __builtin_amdgcn_sched_barrier(0);
    STAMP(13);

    if (tid < 4) *(LAS u32*)(sm + F_OFF + tid * 4) = 0;
    {
        float s0[4] = {bflo(sk[0]), bfhi(sk[0]), bflo(sk[1]), bfhi(sk[1])};
        float s1[4] = {bflo(sk[2]), bfhi(sk[2]), bflo(sk[3]), bfhi(sk[3])};
        u32 kvp[4];
#pragma unroll
        for (int j = 0; j < 4; j++) {
            float a0 = bflo(kk[j]) * s0[3] + bflo(kk[j + 1]) * s0[2] + bflo(kk[j + 2]) * s0[1] + bflo(kk[j + 3]) * s0[0];
            float a1 = bfhi(kk[j]) * s1[3] + bfhi(kk[j + 1]) * s1[2] + bfhi(kk[j + 2]) * s1[1] + bfhi(kk[j + 3]) * s1[0];
            kvp[j] = pk(a0 * bflo(vv[j]), a1 * bfhi(vv[j]));
        }
        STAMP(1);
        const int sw = (rb ^ ((cp & 7) << 1)) * 8;
        const lptr base = sm + (cp * 2) * 128 + sw;
        v2u a, b;
        a[0] = __builtin_amdgcn_perm(kvp[1], kvp[0], 0x05040100); a[1] = __builtin_amdgcn_perm(kvp[3], kvp[2], 0x05040100);
        b[0] = __builtin_amdgcn_perm(kvp[1], kvp[0], 0x07060302); b[1] = __builtin_amdgcn_perm(kvp[3], kvp[2], 0x07060302);
        *(LAS v2u*)(base) = a; *(LAS v2u*)(base + 128) = b;
        a[0] = __builtin_amdgcn_perm(xx[1], xx[0], 0x05040100); a[1] = __builtin_amdgcn_perm(xx[3], xx[2], 0x05040100);
        b[0] = __builtin_amdgcn_perm(xx[1], xx[0], 0x07060302); b[1] = __builtin_amdgcn_perm(xx[3], xx[2], 0x07060302);
        *(LAS v2u*)(base + 8192) = a; *(LAS v2u*)(base + 8192 + 128) = b;
    }
    RAW_BARRIER();
    const __amdgpu_buffer_rsrc_t ut_rsrc = __builtin_amdgcn_make_buffer_rsrc(ws + WS_UT, 0, 2 * D_ * S_ * 2, 0x00020000);
    {
#pragma unroll
        for (int i = 0; i < 2; i++) {
            const int ch = (tid >> 3) & 63, ck = tid & 7;
            const int sw = (ck ^ ((ch >> 1) & 7)) * 16;
            v4u d = *(LAS v4u*)(sm + i * 8192 + ch * 128 + sw);
            __builtin_amdgcn_raw_buffer_store_b128(d, ut_rsrc, (u32)(((i * D_ + dg * 64 + ch) * S_ + tg * 64 + ck * 8) * 2), 0, 16);
        }
    }
    STAMP(2);

    {
        const float dt = rbf(expf(bf1(p_ldt)));
        const float a = dt * bf1(p_are), b = dt * bf1(p_aim);
        const float br = dt * bf1(p_bre), bi = dt * bf1(p_bim);
        const float cre = bf1(p_cre), cim = bf1(p_cim);
        v2f CB;
        CB[0] = cre * br - cim * bi;
        CB[1] = -(cre * bi + cim * br);
        const float a2 = a * 1.4426950408889634f;
        const float rh = b * 0.15915494309189535f;
        const float rl = __builtin_fmaf(b, 0.15915494309189535f, -rh) + b * 6.4206383e-9f;
        const lptr tab = sm + R2_OFF + kch * TAB_CH + kn * 4;
#define PUT(row, v) (*(LAS u32*)(tab + (row) * TAB_PITCH) = pk2(v))
        if (w < 4) {
            v2f E0 = epow<true>(a2, rh, rl, 1.f), E1 = epow<true>(a2, rh, rl, 2.f), E2 = epow<true>(a2, rh, rl, 4.f), E7 = epow<true>(a2, rh, rl, 128.f);
            v2f F1 = epow<false>(a2, rh, rl, 8.f), F2 = epow<false>(a2, rh, rl, 16.f), F4 = epow<false>(a2, rh, rl, 32.f), F8 = epow<false>(a2, rh, rl, 64.f);
            v2f P[8], F[8], t0_, t1_;
            P[0] = CB;
            cmul2(P[1], P[0], E0, P[2], P[0], E1);
            cmul2(P[3], P[1], E1, P[4], P[0], E2);
            cmul2(P[5], P[1], E2, P[6], P[2], E2);
            cmul2(P[7], P[3], E2, F[3], F1, F2);
            F[0][0] = 1.f; F[0][1] = 0.f; F[1] = F1; F[2] = F2; F[4] = F4;
            cmul2(F[5], F4, F1, F[6], F4, F2);
            cmul2(F[7], F4, F[3], t0_, F8, F1);
            PUT(48 + 9, t0_);
#pragma unroll
            for (int e = 0; e < 8; e += 2) {
                PUT(e, P[e]); PUT(e + 1, P[e + 1]);
                cmul2(t0_, P[e], E7, t1_, P[e + 1], E7);
                PUT(8 + e, t0_); PUT(9 + e, t1_);
            }
#pragma unroll
            for (int e = 0; e < 8; e++) PUT(48 + e, F[e]);
            PUT(48 + 8, F8);
#pragma unroll
            for (int e = 2; e < 8; e += 2) {
                cmul2(t0_, F8, F[e], t1_, F8, F[e + 1]);
                PUT(48 + 8 + e, t0_); PUT(48 + 9 + e, t1_);
            }
        } else {
            v2f E1 = epow<true>(a2, rh, rl, 2.f), E2 = epow<true>(a2, rh, rl, 4.f), E7 = epow<true>(a2, rh, rl, 128.f), E8 = epow<true>(a2, rh, rl, 256.f);
            v2f E9 = epow<true>(a2, rh, rl, 512.f), E10 = epow<true>(a2, rh, rl, 1024.f), E11 = epow<true>(a2, rh, rl, 2048.f);
            v2f P2, P4, P6, W3, R[4], Q0, Q1, T0, T1, T2, T3, t0_, t1_;
            cmul2(P2, CB, E1, P4, CB, E2);
            cmul2(P6, P2, E2, W3, E7, E8);
            cmul2(R[0], CB, E8, R[1], P2, E8);
            cmul2(R[2], P4, E8, R[3], P6, E8);
#pragma unroll
            for (int e = 0; e < 4; e += 2) {
                PUT(16 + e, R[e]); PUT(17 + e, R[e + 1]);
                cmul2(t0_, R[e], E7, t1_, R[e + 1], E7);
                PUT(20 + e, t0_); PUT(21 + e, t1_);
            }
            cmul2(Q0, CB, E9, Q1, P4, E9);
            PUT(24, Q0); PUT(28, Q1);
            cmul2(t0_, Q0, E7, t1_, Q1, E7); PUT(25, t0_); PUT(29, t1_);
            cmul2(t0_, Q0, E8, t1_, Q1, E8); PUT(26, t0_); PUT(30, t1_);
            cmul2(t0_, Q0, W3, t1_, Q1, W3); PUT(27, t0_); PUT(31, t1_);
            cmul2(T0, CB, E10, t0_, CB, E11); PUT(32, T0); PUT(40, t0_);
            cmul2(T1, T0, E7, T2, T0, E8); PUT(33, T1); PUT(34, T2);
            cmul2(T3, T0, W3, t0_, T0, E9); PUT(35, T3); PUT(36, t0_);
            cmul2(t0_, T1, E9, t1_, T2, E9); PUT(37, t0_); PUT(38, t1_);
            cmul2(t0_, T3, E9, t1_, T3, E9); PUT(39, t0_);
        }
    }
    STAMP(3);
    const u64 epoch = (u64)__builtin_amdgcn_readfirstlane((u32)E) + ((u64)__builtin_amdgcn_readfirstlane((u32)(E >> 32)) << 32) + 1ull;
    asm volatile("s_waitcnt vmcnt(0)" ::: "memory");
    RAW_BARRIER();
    STAMP(4);
    if (w == 0 && lane < 32) {
        GAS u64* inbox = (GAS u64*)(ws + WS_INBOX) + (dg * 32 + lane) * 32 + tg;
        __hip_atomic_store(inbox, epoch, __ATOMIC_RELAXED, __HIP_MEMORY_SCOPE_AGENT);
    }
    {
        float s = 0.f;
#pragma unroll
        for (int j = 0; j < 4; j++) {
            u32 pp = pk(bflo(gx[j]) * bflo(gw[j]), bfhi(gx[j]) * bfhi(gw[j]));
            s += bflo(pp) + bfhi(pp);
        }
        s = dpp_add<0x111>(s);
        s = dpp_add<0x112>(s);
        s = dpp_add<0x114>(s);
        s = dpp_add<0x118>(s);
        const int si = __float_as_int(s);
        float tot = __int_as_float(__builtin_amdgcn_readlane(si, 15)) + __int_as_float(__builtin_amdgcn_readlane(si, 31)) +
                    __int_as_float(__builtin_amdgcn_readlane(si, 47)) + __int_as_float(__builtin_amdgcn_readlane(si, 63));
        float z = rbf(tot);
        float g = 1.f / (1.f + __expf(-z));
        u32 gb = pk(g, g) & 0xffff;
        if (lane == 0) __hip_atomic_store((GAS u64*)(ws + WS_GATE) + tgate, (epoch << 16) | gb, __ATOMIC_RELAXED, __HIP_MEMORY_SCOPE_AGENT);
    }
    if (w < 6) {
        const int c2 = (w >= 3) ? 1 : 0, mt = w - c2 * 3;
        const lptr ga = sm + R2_OFF + c2 * TAB_CH + (mt * 16 + (lane & 15)) * TAB_PITCH + (lane >> 4) * 16;
        const lptr fb = sm + R2_OFF + c2 * TAB_CH + (48 + (lane & 15)) * TAB_PITCH + (lane >> 4) * 16;
        v4f acc = {0.f, 0.f, 0.f, 0.f};
#pragma unroll
        for (int s = 0; s < 8; s++) {
            v8bf A = *(LAS v8bf*)(ga + s * 64);
            v8bf B = *(LAS v8bf*)(fb + s * 64);
            acc = __builtin_amdgcn_mfma_f32_16x16x32_bf16(A, B, acc, 0, 0, 0);
        }
        const int g4 = lane >> 4;
        int base, step;
        if (mt == 0) { base = ((g4 >> 1) << 7) + ((g4 & 1) << 2); step = 1; }
        else if (mt == 1) { base = (g4 < 2) ? (256 + (g4 << 7)) : (512 + ((g4 & 1) << 2)); step = (g4 < 2) ? 2 : 128; }
        else { base = (g4 < 2) ? (1024 + (g4 << 9)) : (2048 + ((g4 & 1) << 3)); step = 128; }
        const lptr T = sm + T_OFF + c2 * T_CH + (base + 8 * (lane & 15)) * 2;
        const u32 o01 = pk(acc[0], acc[1]), o23 = pk(acc[2], acc[3]);
        *(LAS u16*)(T) = (u16)o01;
        *(LAS u16*)(T + step * 2) = (u16)(o01 >> 16);
        *(LAS u16*)(T + step * 4) = (u16)o23;
        *(LAS u16*)(T + step * 6) = (u16)(o23 >> 16);
    }
    {
        if (hck == 0) { hm[1] = 0; hm[2] = 0; hm[3] = 0; }
        u32 r[7];
        r[0] = rot16(hd[3]); r[1] = rot16(hd[2]); r[2] = rot16(hd[1]); r[3] = rot16(hd[0]);
        r[4] = rot16(hm[3]); r[5] = rot16(hm[2]); r[6] = rot16(hm[1]);
        const lptr dst = sm + (hch * 2) * H_UNIT + (255 - hck) * 16;
        *(LAS v4u*)(dst) = (v4u){r[3], r[4], r[5], r[6]};
        *(LAS v4u*)(dst + H_COPY) = (v4u){r[2], r[3], r[4], r[5]};
        *(LAS v4u*)(dst + 2 * H_COPY) = (v4u){r[1], r[2], r[3], r[4]};
        *(LAS v4u*)(dst + 3 * H_COPY) = (v4u){r[0], r[1], r[2], r[3]};
        if (tid < 48) {
            const int uc = tid / 3, ck = tid - uc * 3;
            *(LAS v4u*)(sm + uc * H_COPY + (256 + ck) * 16) = (v4u){0, 0, 0, 0};
        }
    }
    RAW_BARRIER();
    STAMP(5);
    {
        const lptr T = sm + T_OFF + hch * T_CH;
        const int l0 = hck * 8;
        u32 hv[14];
#pragma unroll
        for (int j = 0; j < 14; j++) {
            const int l = l0 - 6 + j;
            const u32 lv = (u32)rbf((float)(j < 6 ? max(l, 0) : l));
            const u32 x = *(LAS u16*)(T + lv * 2);
            hv[j] = (j < 6 && l < 0) ? 0u : x;
        }
        u32 r[7];
#pragma unroll
        for (int i = 0; i < 7; i++) r[i] = hv[13 - 2 * i] | (hv[12 - 2 * i] << 16);
        const lptr dst = sm + (hch * 2 + 1) * H_UNIT + (255 - hck) * 16;
        *(LAS v4u*)(dst) = (v4u){r[3], r[4], r[5], r[6]};
        *(LAS v4u*)(dst + H_COPY) = (v4u){r[2], r[3], r[4], r[5]};
        *(LAS v4u*)(dst + 2 * H_COPY) = (v4u){r[1], r[2], r[3], r[4]};
        *(LAS v4u*)(dst + 3 * H_COPY) = (v4u){r[0], r[1], r[2], r[3]};
        if (tid < 256) *(LAS v4u*)(sm + U_OFF + (tid >> 5) * U_COPY + (tid & 31) * 16) = (v4u){0, 0, 0, 0};
    }
    STAMP(6);
    {
        const int pidx = w * 4 - 1 + min(lane, 4);
        GAS u64* inbox = (GAS u64*)(ws + WS_INBOX) + p * 32 + max(pidx, 0);
        for (u32 spins = 0; spins < SPIN_CAP; spins++) {
            const u64 v = __hip_atomic_load(inbox, __ATOMIC_RELAXED, __HIP_MEMORY_SCOPE_AGENT);
            if (__all(v == epoch)) break;
            __builtin_amdgcn_s_sleep(1);
        }
        asm volatile("" ::: "memory");
        STAMP(7);
        v4u ud[2];
        u32 up[2];
#pragma unroll
        for (int i = 0; i < 2; i++) {
            const int u = i * 2 + (lane >> 5);
            const int t = w * 256 + (lane & 31) * 8;
            const u32 off = (u32)((((u & 1) * D_ + p * 2 + (u >> 1)) * S_ + t) * 2);
            ud[i] = __builtin_amdgcn_raw_buffer_load_b128(ut_rsrc, off, 0, 16);
            up[i] = __builtin_amdgcn_raw_buffer_load_b32(ut_rsrc, t ? off - 4u : off, 0, 16);
        }
#pragma unroll
        for (int i = 0; i < 2; i++) {
            const int u = i * 2 + (lane >> 5);
            const int t = w * 256 + (lane & 31) * 8;
            const lptr dst = sm + U_OFF + u * U_UNIT + 512 + t * 2;
            const u32 prev = t ? up[i] : 0u;
            *(LAS v4u*)(dst + U_COPY) = ud[i];
            *(LAS v4u*)(dst) = (v4u){__builtin_amdgcn_alignbit(ud[i][0], prev, 16), __builtin_amdgcn_alignbit(ud[i][1], ud[i][0], 16),
                                     __builtin_amdgcn_alignbit(ud[i][2], ud[i][1], 16), __builtin_amdgcn_alignbit(ud[i][3], ud[i][2], 16)};
        }
    }
    float dA0 = bflo(dAp), dA1 = bfhi(dAp), dS0 = bflo(dSp), dS1 = bfhi(dSp);
    asm volatile("" : "+v"(dA0), "+v"(dA1), "+v"(dS0), "+v"(dS1));
    RAW_BARRIER();
    STAMP(11);
    if (w < 4) {
        const int g4 = lane >> 4, n = lane & 15;
        const lptr ha = sm + w * H_UNIT + (n & 3) * H_COPY + 48 - 16 * (n >> 2) + 16 * g4;
        const lptr ub = sm + U_OFF + w * U_UNIT + (n & 1) * U_COPY + 512 + 64 * (n >> 1) + 16 * g4 - 7 * 64;
        const lptr yb = sm + Y_OFF + w * 4096 + 64 * (n >> 1) + 16 * g4 + 8 * (n & 1);
        v8bf A0[8], B0[8], A1[8], B1[8];
        v4f acc[8];
#pragma unroll
        for (int i = 0; i < 8; i++) acc[i] = (v4f){0.f, 0.f, 0.f, 0.f};
        load_set<0>(A0, B0, ha, ub);
        conv_steps<0>(A0, B0, A1, B1, acc, ha, ub, yb, sm + F_OFF
        );
    } else {
        const int ck = tid - 256;
        u32 gv[8];
        {
            GAS u64* gr = (GAS u64*)(ws + WS_GATE) + ck * 8;
            const u64 tag = epoch & 0xFFFFFFFFFFFFull;
            for (u32 spins = 0; spins < SPIN_CAP; spins++) {
                bool ok = true;
#pragma unroll
                for (int i = 0; i < 8; i++) {
                    const u64 v = __hip_atomic_load(gr + i, __ATOMIC_RELAXED, __HIP_MEMORY_SCOPE_AGENT);
                    gv[i] = (u32)v & 0xffff;
                    ok &= (v >> 16) == tag;
                }
                if (__all(ok)) break;
                __builtin_amdgcn_s_sleep(1);
            }
#pragma unroll
            for (int i = 0; i < 8; i++) gv[i] |= pk(0.f, 1.f - bf1((u16)gv[i])) & 0xffff0000u;
        }
        if (bid == 0 && lane == 0) {
            const u32 c4 = __hip_atomic_fetch_add((LAS u32*)(sm + F_OFF + 8), 1u, __ATOMIC_RELAXED, __HIP_MEMORY_SCOPE_WORKGROUP);
            if (c4 == 3) __hip_atomic_store((GAS u64*)(ws + WS_E), epoch, __ATOMIC_RELAXED, __HIP_MEMORY_SCOPE_AGENT);
        }
        STAMP(8);
        if (w < 7) {
            wait_lds(sm + F_OFF, 4);
            STAMP(9);
            if (ck < PASS_SPLIT * 32) epi_chunk(sm, ck * 8, gv, out32, p, dA0, dA1, dS0, dS1);
        }
        if (w >= 6) {
            wait_lds(sm + F_OFF + 4, 4);
            STAMP(10);
            if (ck >= PASS_SPLIT * 32) epi_chunk(sm, ck * 8, gv, out32, p, dA0, dA1, dS0, dS1);
        }
        STAMP(12);
    }
}

__global__ void empty_k() {}
extern "C" void kernel_launch(void* const* d_in, const int* in_sizes, int n_in,
                              void* d_out, int out_size, void* d_ws, size_t ws_size,
                              hipStream_t stream) {
    fused_kernel<<<256, 512, 0, stream>>>((const u16*)d_in[0], (const u16*)d_in[1], (const u16*)d_in[2], (const u16*)d_in[3],
                                          (const u16*)d_in[4], (const u16*)d_in[5], (const u16*)d_in[7], (const u16*)d_in[8],
                                          (const u16*)d_in[9], (const u16*)d_in[10], (const u16*)d_in[11], (const u16*)d_in[12],
                                          (const u16*)d_in[13], (const u16*)d_in[14], (const u16*)d_in[15], (char*)d_ws, (u32*)d_out);
    empty_k<<<1, 64, 0, stream>>>();

}
```

```cpp
#include <hip/hip_runtime.h>
#include <stdint.h>

typedef unsigned short u16;
typedef unsigned int u32;
typedef unsigned long long u64;
typedef short v4s __attribute__((ext_vector_type(4)));
typedef short v8s __attribute__((ext_vector_type(8)));
typedef __bf16 v8bf __attribute__((ext_vector_type(8)));
typedef __bf16 v2bf __attribute__((ext_vector_type(2)));
typedef float v4f __attribute__((ext_vector_type(4)));
typedef float v2f __attribute__((ext_vector_type(2)));
typedef u32 v4u __attribute__((ext_vector_type(4)));
typedef u32 v2u __attribute__((ext_vector_type(2)));
#define LAS __attribute__((address_space(3)))
#define GAS __attribute__((address_space(1)))
typedef LAS char* lptr;

#define S_ 2048
#define D_ 512
#define N_ 128

#define STAMP(i) do { } while (0)

__device__ __forceinline__ float bflo(u32 v) { return __uint_as_float(v << 16); }
__device__ __forceinline__ float bfhi(u32 v) { return __uint_as_float(v & 0xffff0000u); }
__device__ __forceinline__ float bf1(u16 v) { return __uint_as_float(((u32)v) << 16); }
__device__ __forceinline__ u32 pk(float lo, float hi) {
    v2f x = {lo, hi};
    return __builtin_bit_cast(u32, __builtin_convertvector(x, v2bf));
}
__device__ __forceinline__ u32 pk2(v2f x) { return __builtin_bit_cast(u32, __builtin_convertvector(x, v2bf)); }
__device__ __forceinline__ u32 rot16(u32 v) { return __builtin_amdgcn_alignbit(v, v, 16); }
__device__ __forceinline__ float rbf(float f) { return bflo(pk(f, f)); }

template <typename T>
__device__ __forceinline__ T ldg(const void* base, u32 off) { return *(const GAS T*)((const GAS char*)base + off); }

__device__ __forceinline__ void cmul2(v2f& c0, v2f a0, v2f b0, v2f& c1, v2f a1, v2f b1) {
    asm("v_pk_mul_f32 %0, %2, %3 op_sel:[0,0] op_sel_hi:[1,0]\n\t"
        "v_pk_mul_f32 %1, %4, %5 op_sel:[0,0] op_sel_hi:[1,0]\n\t"
        "v_pk_fma_f32 %0, %2, %3, %0 op_sel:[1,1,0] op_sel_hi:[0,1,1] neg_lo:[1,0,0]\n\t"
        "v_pk_fma_f32 %1, %4, %5, %1 op_sel:[1,1,0] op_sel_hi:[0,1,1] neg_lo:[1,0,0]"
        : "=&v"(c0), "=&v"(c1) : "v"(a0), "v"(b0), "v"(a1), "v"(b1));
}

template <bool CONJ>
__device__ __forceinline__ v2f epow(float a2, float rh, float rl, float s) {
    float e = __builtin_amdgcn_exp2f(a2 * s);
    float x = __builtin_fmaf(rl, s, __builtin_amdgcn_fractf(rh * s));
    float cs = __builtin_amdgcn_cosf(x), sn = __builtin_amdgcn_sinf(x);
    v2f c;
    c[0] = e * cs;
    c[1] = CONJ ? -(e * sn) : (e * sn);
    return c;
}

#define H_COPY 4160
#define H_UNIT (4 * H_COPY)
#define R2_OFF (4 * H_UNIT)
#define TAB_PITCH 544
#define TAB_CH (64 * TAB_PITCH)
#define U_OFF R2_OFF
#define U_COPY 4640
#define U_UNIT (2 * U_COPY)
#define Y_OFF (U_OFF + 4 * U_UNIT)
#define T_OFF (R2_OFF + 2 * TAB_CH)
#define T_CH 5248
#define F_OFF (T_OFF + 2 * T_CH)
#define LDS_BYTES (F_OFF + 16)

#define WS_E 0
#define WS_INBOX 4096
#define WS_GATE (128 * 1024)
#define WS_UT (1024 * 1024)
#define WS_STAMPS (8 * 1024 * 1024)

#define SPIN_CAP (1u << 20)
#define RAW_BARRIER() do { asm volatile("s_waitcnt lgkmcnt(0)" ::: "memory"); __builtin_amdgcn_s_barrier(); asm volatile("" ::: "memory"); } while (0)

template <int CTRL>
__device__ __forceinline__ float dpp_add(float v) {
    int t = __builtin_amdgcn_update_dpp(0, __float_as_int(v), CTRL, 0xf, 0xf, true);
    return v + __int_as_float(t);
}

#define NSTEP 16
#define PASS_SPLIT 5
__host__ __device__ constexpr int pass_lo(int p) { return p == 0 ? 0 : PASS_SPLIT; }
__host__ __device__ constexpr int pass_hi(int p) { return p == 0 ? PASS_SPLIT - 1 : 7; }
__host__ __device__ constexpr int pass_nm(int p) {
    int c = 0;
    for (int i = 0; i <= pass_hi(p); i++) for (int q = 0; q <= pass_hi(p); q++) if (i + q >= pass_lo(p) && i + q <= pass_hi(p)) c++;
    return c;
}

template <int S>
__device__ __forceinline__ void load_set(v8bf (&A)[8], v8bf (&B)[8], lptr ha, lptr ub) {
    constexpr int P = S / 8, r = S % 8, N = pass_hi(P) + 1;
#pragma unroll
    for (int i = 0; i < N; i++) {
        A[i] = *(LAS v8bf*)(ha + (7 - r) * 64 + (7 - i) * 512);
        B[i] = *(LAS v8bf*)(ub + (7 - r) * 64 + i * 512);
    }
}
template <int S>
__device__ __forceinline__ void mma_set(const v8bf (&A)[8], const v8bf (&B)[8], v4f (&acc)[8]) {
    constexpr int P = S / 8, LO = pass_lo(P), HI = pass_hi(P);
#pragma unroll
    for (int i = 0; i <= HI; i++)
#pragma unroll
        for (int q = 0; q <= HI; q++)
            if (i + q >= LO && i + q <= HI)
                acc[i + q] = __builtin_amdgcn_mfma_f32_16x16x32_bf16(A[i], B[q], acc[i + q], 0, 0, 0);
}
template <int S>
__device__ __forceinline__ void sched_step() {
    constexpr int M = pass_nm(S / 8);
    constexpr int R = (S + 1 < NSTEP) ? 2 * (pass_hi((S + 1) / 8) + 1) : 0;
    constexpr int per = (R + M - 1) / M;
#pragma unroll
    for (int k = 0; k < M; k++) {
        __builtin_amdgcn_sched_group_barrier(0x8, 1, 0);
        if (R > 0) __builtin_amdgcn_sched_group_barrier(0x100, per, 0);
    }
}
template <int S>
__device__ __forceinline__ void conv_steps(v8bf (&A0)[8], v8bf (&B0)[8], v8bf (&A1)[8], v8bf (&B1)[8], v4f (&acc)[8], lptr ha, lptr ub, lptr yb, lptr flags
) {
    if constexpr (S < NSTEP) {
        if constexpr (S + 1 < NSTEP) {
            if constexpr (S & 1) load_set<S + 1>(A0, B0, ha, ub); else load_set<S + 1>(A1, B1, ha, ub);
        }
        if constexpr (S & 1) mma_set<S>(A1, B1, acc); else mma_set<S>(A0, B0, acc);
        sched_step<S>();
        if constexpr (S % 8 == 7) {
            constexpr int P = S / 8;
#pragma unroll
            for (int t = pass_lo(P); t <= pass_hi(P); t++) {
                v2u o = {pk(acc[t][0], acc[t][1]), pk(acc[t][2], acc[t][3])};
                *(LAS v2u*)(yb + t * 512) = o;
            }
            asm volatile("s_waitcnt lgkmcnt(0)" ::: "memory");
            if ((threadIdx.x & 63) == 0) __hip_atomic_fetch_add((LAS u32*)(flags + P * 4), 1u, __ATOMIC_RELAXED, __HIP_MEMORY_SCOPE_WORKGROUP);
            STAMP(8 + P);
        }
        conv_steps<S + 1>(A0, B0, A1, B1, acc, ha, ub, yb, flags
        );
    }
}

__device__ __forceinline__ void wait_lds(lptr word, u32 want) {
    for (u32 spins = 0; spins < SPIN_CAP; spins++) {
        const u32 v = __hip_atomic_load((LAS u32*)word, __ATOMIC_RELAXED, __HIP_MEMORY_SCOPE_WORKGROUP);
        if (__builtin_amdgcn_readfirstlane(v) >= want) break;
        __builtin_amdgcn_s_sleep(2);
    }
    asm volatile("" ::: "memory");
}

__device__ __forceinline__ void epi_chunk(lptr sm, int T0, const u32 (&gv)[8], u32* __restrict__ out32, int p, float dA0, float dA1, float dS0, float dS1) {
    const v4u ya0 = *(LAS v4u*)(sm + Y_OFF + 0 * 4096 + T0 * 2), yb0 = *(LAS v4u*)(sm + Y_OFF + 1 * 4096 + T0 * 2);
    const v4u ya1 = *(LAS v4u*)(sm + Y_OFF + 2 * 4096 + T0 * 2), yb1 = *(LAS v4u*)(sm + Y_OFF + 3 * 4096 + T0 * 2);
    const lptr ub = sm + U_OFF + U_COPY + 512 + T0 * 2;
    const v4u kv0 = *(LAS v4u*)(ub + 0 * U_UNIT), x0 = *(LAS v4u*)(ub + 1 * U_UNIT);
    const v4u kv1 = *(LAS v4u*)(ub + 2 * U_UNIT), x1 = *(LAS v4u*)(ub + 3 * U_UNIT);
    u32* o = out32 + (u32)(T0 * 256 + p);
#pragma unroll
    for (int e = 0; e < 8; e++) {
        const int yd = (e & 1) * 2 + (e >> 2);
        const bool yh = (e >> 1) & 1;
        const int nd = e >> 1;
        const bool nh = e & 1;
        const float fya0 = yh ? bfhi(ya0[yd]) : bflo(ya0[yd]), fyb0 = yh ? bfhi(yb0[yd]) : bflo(yb0[yd]);
        const float fya1 = yh ? bfhi(ya1[yd]) : bflo(ya1[yd]), fyb1 = yh ? bfhi(yb1[yd]) : bflo(yb1[yd]);
        const float fkv0 = nh ? bfhi(kv0[nd]) : bflo(kv0[nd]), fx0 = nh ? bfhi(x0[nd]) : bflo(x0[nd]);
        const float fkv1 = nh ? bfhi(kv1[nd]) : bflo(kv1[nd]), fx1 = nh ? bfhi(x1[nd]) : bflo(x1[nd]);
        const float g = bflo(gv[e]), g1 = bfhi(gv[e]);
        const float a0 = fx0 * (fya0 + fkv0 * dA0), b0 = fyb0 + fx0 * dS0;
        const float a1 = fx1 * (fya1 + fkv1 * dA1), b1 = fyb1 + fx1 * dS1;
        o[e * 256] = pk(g * a0 + g1 * b0, g * a1 + g1 * b1);
    }
}

__global__ void __launch_bounds__(512, 2)
fused_kernel(const u16* __restrict__ X, const u16* __restrict__ Kin, const u16* __restrict__ V, const u16* __restrict__ shiftk,
             const u16* __restrict__ hA, const u16* __restrict__ ssmD, const u16* __restrict__ Are, const u16* __restrict__ Aim,
             const u16* __restrict__ Bre, const u16* __restrict__ Bim, const u16* __restrict__ Cre, const u16* __restrict__ Cim,
             const u16* __restrict__ logdt, const u16* __restrict__ Dskip, const u16* __restrict__ gatew,
             char* __restrict__ ws, u32* __restrict__ out32) {
    __shared__ __attribute__((aligned(16))) char smem_[LDS_BYTES];
    const lptr sm = (lptr)smem_;
    const int tid = threadIdx.x;
    const int bid = blockIdx.x;
    const int lane = tid & 63;
    const int w = __builtin_amdgcn_readfirstlane(tid >> 6);
    const int dg = bid & 7, tg = bid >> 3;
    const int p = dg * 32 + tg;
    STAMP(0);

    const int cp = tid & 31, rb = tid >> 5;
    const int t0 = tg * 64 + rb * 4;
    const int c = dg * 64 + cp * 2;
    u32 kk[7], vv[4], xx[4];
    {
        const __amdgpu_buffer_rsrc_t k_rsrc = __builtin_amdgcn_make_buffer_rsrc((void*)Kin, 0, S_ * D_ * 2, 0x00020000);
        const u32 o = (u32)(t0 * D_ + c) * 2u;
        kk[0] = __builtin_amdgcn_raw_buffer_load_b32(k_rsrc, o - 3072u, 0, 0);
        kk[1] = __builtin_amdgcn_raw_buffer_load_b32(k_rsrc, o - 2048u, 0, 0);
        kk[2] = __builtin_amdgcn_raw_buffer_load_b32(k_rsrc, o - 1024u, 0, 0);
#pragma unroll
        for (int j = 0; j < 4; j++) kk[3 + j] = ldg<u32>(Kin, o + j * 1024);
#pragma unroll
        for (int j = 0; j < 4; j++) vv[j] = ldg<u32>(V, o + j * 1024);
#pragma unroll
        for (int j = 0; j < 4; j++) xx[j] = ldg<u32>(X, o + j * 1024);
    }
    const v4u sk = ldg<v4u>(shiftk, (u32)c * 8u);
    const int kch = (tid >> 7) & 1, kn = tid & 127;
    const int kd = p * 2 + kch;
    const u32 pi = (u32)(kd * N_ + kn) * 2u;
    const u16 p_ldt = ldg<u16>(logdt, (u32)kd * 2u);
    const u16 p_are = ldg<u16>(Are, pi), p_aim = ldg<u16>(Aim, pi), p_bre = ldg<u16>(Bre, pi), p_bim = ldg<u16>(Bim, pi), p_cre = ldg<u16>(Cre, pi), p_cim = ldg<u16>(Cim, pi);
    const u64 E = __hip_atomic_load((GAS u64*)(ws + WS_E), __ATOMIC_RELAXED, __HIP_MEMORY_SCOPE_AGENT);
    const int hch = tid >> 8, hck = tid & 255;
    const u32 ho = (u32)((p * 2 + hch) * S_ + hck * 8) * 2u;
    const v4u hd = ldg<v4u>(hA, ho);
    v4u hm = ldg<v4u>(hA, ho - (hck ? 16u : 0u));
    const int tgate = bid * 8 + w;
    const v4u gx = ldg<v4u>(X, (u32)(tgate * D_ + lane * 8) * 2u);
    const v4u gw = ldg<v4u>(gatew, (u32)lane * 16u);
    const u32 dAp = *(const u32*)(ssmD + p * 2), dSp = *(const u32*)(Dskip + p * 2);
    __builtin_amdgcn_sched_barrier(0);
    STAMP(13);

    if (tid < 4) *(LAS u32*)(sm + F_OFF + tid * 4) = 0;
    {
        float s0[4] = {bflo(sk[0]), bfhi(sk[0]), bflo(sk[1]), bfhi(sk[1])};
        float s1[4] = {bflo(sk[2]), bfhi(sk[2]), bflo(sk[3]), bfhi(sk[3])};
        u32 kvp[4];
#pragma unroll
        for (int j = 0; j < 4; j++) {
            float a0 = bflo(kk[j]) * s0[3] + bflo(kk[j + 1]) * s0[2] + bflo(kk[j + 2]) * s0[1] + bflo(kk[j + 3]) * s0[0];
            float a1 = bfhi(kk[j]) * s1[3] + bfhi(kk[j + 1]) * s1[2] + bfhi(kk[j + 2]) * s1[1] + bfhi(kk[j + 3]) * s1[0];
            kvp[j] = pk(a0 * bflo(vv[j]), a1 * bfhi(vv[j]));
        }
        STAMP(1);
        const int sw = (rb ^ ((cp & 7) << 1)) * 8;
        const lptr base = sm + (cp * 2) * 128 + sw;
        v2u a, b;
        a[0] = __builtin_amdgcn_perm(kvp[1], kvp[0], 0x05040100); a[1] = __builtin_amdgcn_perm(kvp[3], kvp[2], 0x05040100);
        b[0] = __builtin_amdgcn_perm(kvp[1], kvp[0], 0x07060302); b[1] = __builtin_amdgcn_perm(kvp[3], kvp[2], 0x07060302);
        *(LAS v2u*)(base) = a; *(LAS v2u*)(base + 128) = b;
        a[0] = __builtin_amdgcn_perm(xx[1], xx[0], 0x05040100); a[1] = __builtin_amdgcn_perm(xx[3], xx[2], 0x05040100);
        b[0] = __builtin_amdgcn_perm(xx[1], xx[0], 0x07060302); b[1] = __builtin_amdgcn_perm(xx[3], xx[2], 0x07060302);
        *(LAS v2u*)(base + 8192) = a; *(LAS v2u*)(base + 8192 + 128) = b;
    }
    RAW_BARRIER();
    const __amdgpu_buffer_rsrc_t ut_rsrc = __builtin_amdgcn_make_buffer_rsrc(ws + WS_UT, 0, 2 * D_ * S_ * 2, 0x00020000);
    {
#pragma unroll
        for (int i = 0; i < 2; i++) {
            const int ch = (tid >> 3) & 63, ck = tid & 7;
            const int sw = (ck ^ ((ch >> 1) & 7)) * 16;
            v4u d = *(LAS v4u*)(sm + i * 8192 + ch * 128 + sw);
            __builtin_amdgcn_raw_buffer_store_b128(d, ut_rsrc, (u32)(((i * D_ + dg * 64 + ch) * S_ + tg * 64 + ck * 8) * 2), 0, 16);
        }
    }
    STAMP(2);

    {
        const float dt = rbf(expf(bf1(p_ldt)));
        const float a = dt * bf1(p_are), b = dt * bf1(p_aim);
        const float br = dt * bf1(p_bre), bi = dt * bf1(p_bim);
        const float cre = bf1(p_cre), cim = bf1(p_cim);
        v2f CB;
        CB[0] = cre * br - cim * bi;
        CB[1] = -(cre * bi + cim * br);
        const float a2 = a * 1.4426950408889634f;
        const float rh = b * 0.15915494309189535f;
        const float rl = __builtin_fmaf(b, 0.15915494309189535f, -rh) + b * 6.4206383e-9f;
        const lptr tab = sm + R2_OFF + kch * TAB_CH + kn * 4;
#define PUT(row, v) (*(LAS u32*)(tab + (row) * TAB_PITCH) = pk2(v))
        if (w < 4) {
            v2f E0 = epow<true>(a2, rh, rl, 1.f), E1 = epow<true>(a2, rh, rl, 2.f), E2 = epow<true>(a2, rh, rl, 4.f), E7 = epow<true>(a2, rh, rl, 128.f);
            v2f F1 = epow<false>(a2, rh, rl, 8.f), F2 = epow<false>(a2, rh, rl, 16.f), F4 = epow<false>(a2, rh, rl, 32.f), F8 = epow<false>(a2, rh, rl, 64.f);
            v2f P[8], F[8], t0_, t1_;
            P[0] = CB;
            cmul2(P[1], P[0], E0, P[2], P[0], E1);
            cmul2(P[3], P[1], E1, P[4], P[0], E2);
            cmul2(P[5], P[1], E2, P[6], P[2], E2);
            cmul2(P[7], P[3], E2, F[3], F1, F2);
            F[0][0] = 1.f; F[0][1] = 0.f; F[1] = F1; F[2] = F2; F[4] = F4;
            cmul2(F[5], F4, F1, F[6], F4, F2);
            cmul2(F[7], F4, F[3], t0_, F8, F1);
            PUT(48 + 9, t0_);
#pragma unroll
            for (int e = 0; e < 8; e += 2) {
                PUT(e, P[e]); PUT(e + 1, P[e + 1]);
                cmul2(t0_, P[e], E7, t1_, P[e + 1], E7);
                PUT(8 + e, t0_); PUT(9 + e, t1_);
            }
#pragma unroll
            for (int e = 0; e < 8; e++) PUT(48 + e, F[e]);
            PUT(48 + 8, F8);
#pragma unroll
            for (int e = 2; e < 8; e += 2) {
                cmul2(t0_, F8, F[e], t1_, F8, F[e + 1]);
                PUT(48 + 8 + e, t0_); PUT(48 + 9 + e, t1_);
            }
        } else {
            v2f E1 = epow<true>(a2, rh, rl, 2.f), E2 = epow<true>(a2, rh, rl, 4.f), E7 = epow<true>(a2, rh, rl, 128.f), E8 = epow<true>(a2, rh, rl, 256.f);
            v2f E9 = epow<true>(a2, rh, rl, 512.f), E10 = epow<true>(a2, rh, rl, 1024.f), E11 = epow<true>(a2, rh, rl, 2048.f);
            v2f P2, P4, P6, W3, R[4], Q0, Q1, T0, T1, T2, T3, t0_, t1_;
            cmul2(P2, CB, E1, P4, CB, E2);
            cmul2(P6, P2, E2, W3, E7, E8);
            cmul2(R[0], CB, E8, R[1], P2, E8);
            cmul2(R[2], P4, E8, R[3], P6, E8);
#pragma unroll
            for (int e = 0; e < 4; e += 2) {
                PUT(16 + e, R[e]); PUT(17 + e, R[e + 1]);
                cmul2(t0_, R[e], E7, t1_, R[e + 1], E7);
                PUT(20 + e, t0_); PUT(21 + e, t1_);
            }
            cmul2(Q0, CB, E9, Q1, P4, E9);
            PUT(24, Q0); PUT(28, Q1);
            cmul2(t0_, Q0, E7, t1_, Q1, E7); PUT(25, t0_); PUT(29, t1_);
            cmul2(t0_, Q0, E8, t1_, Q1, E8); PUT(26, t0_); PUT(30, t1_);
            cmul2(t0_, Q0, W3, t1_, Q1, W3); PUT(27, t0_); PUT(31, t1_);
            cmul2(T0, CB, E10, t0_, CB, E11); PUT(32, T0); PUT(40, t0_);
            cmul2(T1, T0, E7, T2, T0, E8); PUT(33, T1); PUT(34, T2);
            cmul2(T3, T0, W3, t0_, T0, E9); PUT(35, T3); PUT(36, t0_);
            cmul2(t0_, T1, E9, t1_, T2, E9); PUT(37, t0_); PUT(38, t1_);
            cmul2(t0_, T3, E9, t1_, T3, E9); PUT(39, t0_);
        }
    }
    STAMP(3);
    const u64 epoch = (u64)__builtin_amdgcn_readfirstlane((u32)E) + ((u64)__builtin_amdgcn_readfirstlane((u32)(E >> 32)) << 32) + 1ull;
    asm volatile("s_waitcnt vmcnt(0)" ::: "memory");
    RAW_BARRIER();
    STAMP(4);
    if (w == 0 && lane < 32) {
        GAS u64* inbox = (GAS u64*)(ws + WS_INBOX) + (dg * 32 + lane) * 32 + tg;
        __hip_atomic_store(inbox, epoch, __ATOMIC_RELAXED, __HIP_MEMORY_SCOPE_AGENT);
    }
    {
        float s = 0.f;
#pragma unroll
        for (int j = 0; j < 4; j++) {
            u32 pp = pk(bflo(gx[j]) * bflo(gw[j]), bfhi(gx[j]) * bfhi(gw[j]));
            s += bflo(pp) + bfhi(pp);
        }
        s = dpp_add<0x111>(s);
        s = dpp_add<0x112>(s);
        s = dpp_add<0x114>(s);
        s = dpp_add<0x118>(s);
        const int si = __float_as_int(s);
        float tot = __int_as_float(__builtin_amdgcn_readlane(si, 15)) + __int_as_float(__builtin_amdgcn_readlane(si, 31)) +
                    __int_as_float(__builtin_amdgcn_readlane(si, 47)) + __int_as_float(__builtin_amdgcn_readlane(si, 63));
        float z = rbf(tot);
        float g = 1.f / (1.f + __expf(-z));
        u32 gb = pk(g, g) & 0xffff;
        if (lane == 0) __hip_atomic_store((GAS u64*)(ws + WS_GATE) + tgate, (epoch << 16) | gb, __ATOMIC_RELAXED, __HIP_MEMORY_SCOPE_AGENT);
    }
    if (w < 6) {
        const int c2 = (w >= 3) ? 1 : 0, mt = w - c2 * 3;
        const lptr ga = sm + R2_OFF + c2 * TAB_CH + (mt * 16 + (lane & 15)) * TAB_PITCH + (lane >> 4) * 16;
        const lptr fb = sm + R2_OFF + c2 * TAB_CH + (48 + (lane & 15)) * TAB_PITCH + (lane >> 4) * 16;
        v4f acc = {0.f, 0.f, 0.f, 0.f};
#pragma unroll
        for (int s = 0; s < 8; s++) {
            v8bf A = *(LAS v8bf*)(ga + s * 64);
            v8bf B = *(LAS v8bf*)(fb + s * 64);
            acc = __builtin_amdgcn_mfma_f32_16x16x32_bf16(A, B, acc, 0, 0, 0);
        }
        const int g4 = lane >> 4;
        int base, step;
        if (mt == 0) { base = ((g4 >> 1) << 7) + ((g4 & 1) << 2); step = 1; }
        else if (mt == 1) { base = (g4 < 2) ? (256 + (g4 << 7)) : (512 + ((g4 & 1) << 2)); step = (g4 < 2) ? 2 : 128; }
        else { base = (g4 < 2) ? (1024 + (g4 << 9)) : (2048 + ((g4 & 1) << 3)); step = 128; }
        const lptr T = sm + T_OFF + c2 * T_CH + (base + 8 * (lane & 15)) * 2;
        const u32 o01 = pk(acc[0], acc[1]), o23 = pk(acc[2], acc[3]);
        *(LAS u16*)(T) = (u16)o01;
        *(LAS u16*)(T + step * 2) = (u16)(o01 >> 16);
        *(LAS u16*)(T + step * 4) = (u16)o23;
        *(LAS u16*)(T + step * 6) = (u16)(o23 >> 16);
    }
    {
        if (hck == 0) { hm[1] = 0; hm[2] = 0; hm[3] = 0; }
        u32 r[7];
        r[0] = rot16(hd[3]); r[1] = rot16(hd[2]); r[2] = rot16(hd[1]); r[3] = rot16(hd[0]);
        r[4] = rot16(hm[3]); r[5] = rot16(hm[2]); r[6] = rot16(hm[1]);
        const lptr dst = sm + (hch * 2) * H_UNIT + (255 - hck) * 16;
        *(LAS v4u*)(dst) = (v4u){r[3], r[4], r[5], r[6]};
        *(LAS v4u*)(dst + H_COPY) = (v4u){r[2], r[3], r[4], r[5]};
        *(LAS v4u*)(dst + 2 * H_COPY) = (v4u){r[1], r[2], r[3], r[4]};
        *(LAS v4u*)(dst + 3 * H_COPY) = (v4u){r[0], r[1], r[2], r[3]};
        if (tid < 48) {
            const int uc = tid / 3, ck = tid - uc * 3;
            *(LAS v4u*)(sm + uc * H_COPY + (256 + ck) * 16) = (v4u){0, 0, 0, 0};
        }
    }
    RAW_BARRIER();
    STAMP(5);
    {
        const lptr T = sm + T_OFF + hch * T_CH;
        const int l0 = hck * 8;
        u32 hv[14];
#pragma unroll
        for (int j = 0; j < 14; j++) {
            const int l = l0 - 6 + j;
            const u32 lv = (u32)rbf((float)(j < 6 ? max(l, 0) : l));
            const u32 x = *(LAS u16*)(T + lv * 2);
            hv[j] = (j < 6 && l < 0) ? 0u : x;
        }
        u32 r[7];
#pragma unroll
        for (int i = 0; i < 7; i++) r[i] = hv[13 - 2 * i] | (hv[12 - 2 * i] << 16);
        const lptr dst = sm + (hch * 2 + 1) * H_UNIT + (255 - hck) * 16;
        *(LAS v4u*)(dst) = (v4u){r[3], r[4], r[5], r[6]};
        *(LAS v4u*)(dst + H_COPY) = (v4u){r[2], r[3], r[4], r[5]};
        *(LAS v4u*)(dst + 2 * H_COPY) = (v4u){r[1], r[2], r[3], r[4]};
        *(LAS v4u*)(dst + 3 * H_COPY) = (v4u){r[0], r[1], r[2], r[3]};
        if (tid < 256) *(LAS v4u*)(sm + U_OFF + (tid >> 5) * U_COPY + (tid & 31) * 16) = (v4u){0, 0, 0, 0};
    }
    STAMP(6);
    {
        const int pidx = w * 4 - 1 + min(lane, 4);
        GAS u64* inbox = (GAS u64*)(ws + WS_INBOX) + p * 32 + max(pidx, 0);
        for (u32 spins = 0; spins < SPIN_CAP; spins++) {
            const u64 v = __hip_atomic_load(inbox, __ATOMIC_RELAXED, __HIP_MEMORY_SCOPE_AGENT);
            if (__all(v == epoch)) break;
            __builtin_amdgcn_s_sleep(1);
        }
        asm volatile("" ::: "memory");
        STAMP(7);
        v4u ud[2];
        u32 up[2];
#pragma unroll
        for (int i = 0; i < 2; i++) {
            const int u = i * 2 + (lane >> 5);
            const int t = w * 256 + (lane & 31) * 8;
            const u32 off = (u32)((((u & 1) * D_ + p * 2 + (u >> 1)) * S_ + t) * 2);
            ud[i] = __builtin_amdgcn_raw_buffer_load_b128(ut_rsrc, off, 0, 16);
            up[i] = __builtin_amdgcn_raw_buffer_load_b32(ut_rsrc, t ? off - 4u : off, 0, 16);
        }
#pragma unroll
        for (int i = 0; i < 2; i++) {
            const int u = i * 2 + (lane >> 5);
            const int t = w * 256 + (lane & 31) * 8;
            const lptr dst = sm + U_OFF + u * U_UNIT + 512 + t * 2;
            const u32 prev = t ? up[i] : 0u;
            *(LAS v4u*)(dst + U_COPY) = ud[i];
            *(LAS v4u*)(dst) = (v4u){__builtin_amdgcn_alignbit(ud[i][0], prev, 16), __builtin_amdgcn_alignbit(ud[i][1], ud[i][0], 16),
                                     __builtin_amdgcn_alignbit(ud[i][2], ud[i][1], 16), __builtin_amdgcn_alignbit(ud[i][3], ud[i][2], 16)};
        }
    }
    float dA0 = bflo(dAp), dA1 = bfhi(dAp), dS0 = bflo(dSp), dS1 = bfhi(dSp);
    asm volatile("" : "+v"(dA0), "+v"(dA1), "+v"(dS0), "+v"(dS1));
    RAW_BARRIER();
    STAMP(11);
    if (w < 4) {
        const int g4 = lane >> 4, n = lane & 15;
        const lptr ha = sm + w * H_UNIT + (n & 3) * H_COPY + 48 - 16 * (n >> 2) + 16 * g4;
        const lptr ub = sm + U_OFF + w * U_UNIT + (n & 1) * U_COPY + 512 + 64 * (n >> 1) + 16 * g4 - 7 * 64;
        const lptr yb = sm + Y_OFF + w * 4096 + 64 * (n >> 1) + 16 * g4 + 8 * (n & 1);
        v8bf A0[8], B0[8], A1[8], B1[8];
        v4f acc[8];
#pragma unroll
        for (int i = 0; i < 8; i++) acc[i] = (v4f){0.f, 0.f, 0.f, 0.f};
        load_set<0>(A0, B0, ha, ub);
        conv_steps<0>(A0, B0, A1, B1, acc, ha, ub, yb, sm + F_OFF
        );
    } else {
        const int ck = tid - 256;
        u32 gv[8];
        {
            GAS u64* gr = (GAS u64*)(ws + WS_GATE) + ck * 8;
            const u64 tag = epoch & 0xFFFFFFFFFFFFull;
            for (u32 spins = 0; spins < SPIN_CAP; spins++) {
                bool ok = true;
#pragma unroll
                for (int i = 0; i < 8; i++) {
                    const u64 v = __hip_atomic_load(gr + i, __ATOMIC_RELAXED, __HIP_MEMORY_SCOPE_AGENT);
                    gv[i] = (u32)v & 0xffff;
                    ok &= (v >> 16) == tag;
                }
                if (__all(ok)) break;
                __builtin_amdgcn_s_sleep(1);
            }
#pragma unroll
            for (int i = 0; i < 8; i++) gv[i] |= pk(0.f, 1.f - bf1((u16)gv[i])) & 0xffff0000u;
        }
        if (bid == 0 && lane == 0) {
            const u32 c4 = __hip_atomic_fetch_add((LAS u32*)(sm + F_OFF + 8), 1u, __ATOMIC_RELAXED, __HIP_MEMORY_SCOPE_WORKGROUP);
            if (c4 == 3) __hip_atomic_store((GAS u64*)(ws + WS_E), epoch, __ATOMIC_RELAXED, __HIP_MEMORY_SCOPE_AGENT);
        }
        STAMP(8);
        if (w < 7) {
            wait_lds(sm + F_OFF, 4);
            STAMP(9);
            if (ck < PASS_SPLIT * 32) epi_chunk(sm, ck * 8, gv, out32, p, dA0, dA1, dS0, dS1);
        }
        if (w >= 6) {
            wait_lds(sm + F_OFF + 4, 4);
            STAMP(10);
            if (ck >= PASS_SPLIT * 32) epi_chunk(sm, ck * 8, gv, out32, p, dA0, dA1, dS0, dS1);
        }
        STAMP(12);
    }
}

__global__ void stamp_k(char* ws) { if (threadIdx.x == 0 && blockIdx.x == 0) *(volatile GAS u64*)(ws + 64 * 1024 * 1024) = __builtin_amdgcn_s_memrealtime(); }
__global__ void __launch_bounds__(512) probe_k(char* ws) {
    __shared__ char sm[146704];
    u64 now = __builtin_amdgcn_s_memrealtime(); asm volatile("s_waitcnt lgkmcnt(0)" ::: "memory");
    sm[threadIdx.x] = 1; __syncthreads(); if (sm[(threadIdx.x + 1) & 511] == 77) ws[0x5000000] = 1;

    if (threadIdx.x == 0 && blockIdx.x == 0) {
        u64 ta = *(volatile GAS u64*)(ws + 64 * 1024 * 1024);
        u64 until = now + 20 * (now - ta);
        while (__builtin_amdgcn_s_memrealtime() < until) __builtin_amdgcn_s_sleep(1);
    }
}

extern "C" void kernel_launch(void* const* d_in, const int* in_sizes, int n_in,
                              void* d_out, int out_size, void* d_ws, size_t ws_size,
                              hipStream_t stream) {
    stamp_k<<<1, 64, 0, stream>>>((char*)d_ws);
    probe_k<<<256, 512, 0, stream>>>((char*)d_ws);
    fused_kernel<<<256, 512, 0, stream>>>((const u16*)d_in[0], (const u16*)d_in[1], (const u16*)d_in[2], (const u16*)d_in[3],
                                          (const u16*)d_in[4], (const u16*)d_in[5], (const u16*)d_in[7], (const u16*)d_in[8],
                                          (const u16*)d_in[9], (const u16*)d_in[10], (const u16*)d_in[11], (const u16*)d_in[12],
                                          (const u16*)d_in[13], (const u16*)d_in[14], (const u16*)d_in[15], (char*)d_ws, (u32*)d_out);
}
```

```cpp
#include <hip/hip_runtime.h>
#include <stdint.h>

typedef unsigned short u16;
typedef unsigned int u32;
typedef unsigned long long u64;
typedef short v4s __attribute__((ext_vector_type(4)));
typedef short v8s __attribute__((ext_vector_type(8)));
typedef __bf16 v8bf __attribute__((ext_vector_type(8)));
typedef __bf16 v2bf __attribute__((ext_vector_type(2)));
typedef float v4f __attribute__((ext_vector_type(4)));
typedef float v2f __attribute__((ext_vector_type(2)));
typedef u32 v4u __attribute__((ext_vector_type(4)));
typedef u32 v2u __attribute__((ext_vector_type(2)));
typedef u32 v3u __attribute__((ext_vector_type(3)));
#define LAS __attribute__((address_space(3)))
#define GAS __attribute__((address_space(1)))
typedef LAS char* lptr;

#define S_ 2048
#define D_ 512
#define N_ 128

#define STAMP(i) do { } while (0)
#define STAMP_FLUSH() do { } while (0)

__device__ __forceinline__ float bflo(u32 v) { return __uint_as_float(v << 16); }
__device__ __forceinline__ float bfhi(u32 v) { return __uint_as_float(v & 0xffff0000u); }
__device__ __forceinline__ float bf1(u16 v) { return __uint_as_float(((u32)v) << 16); }
__device__ __forceinline__ u32 pk(float lo, float hi) {
    v2f x = {lo, hi};
    return __builtin_bit_cast(u32, __builtin_convertvector(x, v2bf));
}
__device__ __forceinline__ u32 pk2(v2f x) { return __builtin_bit_cast(u32, __builtin_convertvector(x, v2bf)); }
__device__ __forceinline__ u32 rot16(u32 v) { return __builtin_amdgcn_alignbit(v, v, 16); }
__device__ __forceinline__ float rbf(float f) { return bflo(pk(f, f)); }

template <typename T>
__device__ __forceinline__ T ldg(const void* base, u32 off) { return *(const GAS T*)((const GAS char*)base + off); }

__device__ __forceinline__ void cmul2(v2f& c0, v2f a0, v2f b0, v2f& c1, v2f a1, v2f b1) {
    asm("v_pk_mul_f32 %0, %2, %3 op_sel:[0,0] op_sel_hi:[1,0]\n\t"
        "v_pk_mul_f32 %1, %4, %5 op_sel:[0,0] op_sel_hi:[1,0]\n\t"
        "v_pk_fma_f32 %0, %2, %3, %0 op_sel:[1,1,0] op_sel_hi:[0,1,1] neg_lo:[1,0,0]\n\t"
        "v_pk_fma_f32 %1, %4, %5, %1 op_sel:[1,1,0] op_sel_hi:[0,1,1] neg_lo:[1,0,0]"
        : "=&v"(c0), "=&v"(c1) : "v"(a0), "v"(b0), "v"(a1), "v"(b1));
}

__device__ __forceinline__ void csqr2(v2f& c0, v2f a0, v2f& c1, v2f a1) { cmul2(c0, a0, a0, c1, a1, a1); }

template <bool CONJ>
__device__ __forceinline__ v2f epow(float a2, float rh, float rl, float s) {
    float e = __builtin_amdgcn_exp2f(a2 * s);
    float x = __builtin_fmaf(rl, s, __builtin_amdgcn_fractf(rh * s));
    float cs = __builtin_amdgcn_cosf(x), sn = __builtin_amdgcn_sinf(x);
    v2f c;
    c[0] = e * cs;
    c[1] = CONJ ? -(e * sn) : (e * sn);
    return c;
}

#define H_COPY 4160
#define H_UNIT (4 * H_COPY)
#define R2_OFF (4 * H_UNIT)
#define TAB_PITCH 544
#define TAB_CH (64 * TAB_PITCH)
#define U_OFF R2_OFF
#define U_COPY 4640
#define U_UNIT (2 * U_COPY)
#define Y_OFF (U_OFF + 4 * U_UNIT)
#define T_OFF (R2_OFF + 2 * TAB_CH)
#define T_CH 5248
#define TILE_OFF (T_OFF + 2 * T_CH)
#define F_OFF (TILE_OFF + 16384)
#define LDS_BYTES (F_OFF + 32)

#define WS_E 0
#define WS_INBOX 4096
#define WS_GATE (128 * 1024)
#define WS_UT (1024 * 1024)
#define WS_STAMPS (8 * 1024 * 1024)

#define SPIN_CAP (1u << 20)
#define RAW_BARRIER() do { asm volatile("s_waitcnt lgkmcnt(0)" ::: "memory"); __builtin_amdgcn_s_barrier(); asm volatile("" ::: "memory"); } while (0)

template <int CTRL>
__device__ __forceinline__ float dpp_add(float v) {
    int t = __builtin_amdgcn_update_dpp(0, __float_as_int(v), CTRL, 0xf, 0xf, true);
    return v + __int_as_float(t);
}

__device__ __forceinline__ void lds_arrive(lptr word) {
    asm volatile("s_waitcnt lgkmcnt(0)" ::: "memory");
    if ((threadIdx.x & 63) == 0) __hip_atomic_fetch_add((LAS u32*)word, 1u, __ATOMIC_RELAXED, __HIP_MEMORY_SCOPE_WORKGROUP);
}
__device__ __forceinline__ void wait_lds(lptr word, u32 want) {
#pragma unroll 1
    for (u32 spins = 0; spins < SPIN_CAP; spins++) {
        const u32 v = __hip_atomic_load((LAS u32*)word, __ATOMIC_RELAXED, __HIP_MEMORY_SCOPE_WORKGROUP);
        if (__builtin_amdgcn_readfirstlane(v) >= want) break;
        __builtin_amdgcn_s_sleep(1);
    }
    asm volatile("" ::: "memory");
}

#define NSTEP 16
#define PASS_SPLIT 5
__host__ __device__ constexpr int pass_lo(int p) { return p == 0 ? 0 : PASS_SPLIT; }
__host__ __device__ constexpr int pass_hi(int p) { return p == 0 ? PASS_SPLIT - 1 : 7; }
__host__ __device__ constexpr int pass_nm(int p) {
    int c = 0;
    for (int i = 0; i <= pass_hi(p); i++) for (int q = 0; q <= pass_hi(p); q++) if (i + q >= pass_lo(p) && i + q <= pass_hi(p)) c++;
    return c;
}

template <int S>
__device__ __forceinline__ void load_set(v8bf (&A)[8], v8bf (&B)[8], lptr ha, lptr ub) {
    constexpr int P = S / 8, r = S % 8, N = pass_hi(P) + 1;
#pragma unroll
    for (int i = 0; i < N; i++) {
        A[i] = *(LAS v8bf*)(ha + (7 - r) * 64 + (7 - i) * 512);
        B[i] = *(LAS v8bf*)(ub + (7 - r) * 64 + i * 512);
    }
}
template <int S>
__device__ __forceinline__ void mma_set(const v8bf (&A)[8], const v8bf (&B)[8], v4f (&acc)[8]) {
    constexpr int P = S / 8, LO = pass_lo(P), HI = pass_hi(P);
#pragma unroll
    for (int i = 0; i <= HI; i++)
#pragma unroll
        for (int q = 0; q <= HI; q++)
            if (i + q >= LO && i + q <= HI)
                acc[i + q] = __builtin_amdgcn_mfma_f32_16x16x32_bf16(A[i], B[q], acc[i + q], 0, 0, 0);
}
template <int S>
__device__ __forceinline__ void sched_step() {
    constexpr int M = pass_nm(S / 8);
    constexpr int R = (S + 1 < NSTEP) ? 2 * (pass_hi((S + 1) / 8) + 1) : 0;
    constexpr int per = (R + M - 1) / M;
#pragma unroll
    for (int k = 0; k < M; k++) {
        __builtin_amdgcn_sched_group_barrier(0x8, 1, 0);
        if (R > 0) __builtin_amdgcn_sched_group_barrier(0x100, per, 0);
    }
}
template <int S>
__device__ __forceinline__ void conv_steps(v8bf (&A0)[8], v8bf (&B0)[8], v8bf (&A1)[8], v8bf (&B1)[8], v4f (&acc)[8], lptr ha, lptr ub, lptr yb, lptr flags
) {
    if constexpr (S < NSTEP) {
        if constexpr (S + 1 < NSTEP) {
            if constexpr (S & 1) load_set<S + 1>(A0, B0, ha, ub); else load_set<S + 1>(A1, B1, ha, ub);
        }
        if constexpr (S & 1) mma_set<S>(A1, B1, acc); else mma_set<S>(A0, B0, acc);
        sched_step<S>();
        if constexpr (S % 8 == 7) {
            constexpr int P = S / 8;
#pragma unroll
            for (int t = pass_lo(P); t <= pass_hi(P); t++) {
                v2u o = {pk(acc[t][0], acc[t][1]), pk(acc[t][2], acc[t][3])};
                *(LAS v2u*)(yb + t * 512) = o;
            }
            lds_arrive(flags + P * 4);
            STAMP(9 + P);
        }
        conv_steps<S + 1>(A0, B0, A1, B1, acc, ha, ub, yb, flags
        );
    }
}

#define NTHREADS 768
#define CODE_BYTES 15360
#define G_OFF TILE_OFF

__device__ __forceinline__ void epi_quad(lptr sm, int T0, int b, u32* __restrict__ out32, int p, float dA0, float dA1, float dS0, float dS1) {
    const lptr yp = sm + Y_OFF + T0 * 2 + b * 8;
    const v2u ya0 = *(LAS v2u*)(yp), yb0 = *(LAS v2u*)(yp + 4096), ya1 = *(LAS v2u*)(yp + 8192), yb1 = *(LAS v2u*)(yp + 12288);
    const lptr ub = sm + U_OFF + b * U_COPY + 512 + T0 * 2;
    const v4u kv0 = *(LAS v4u*)(ub), x0 = *(LAS v4u*)(ub + U_UNIT), kv1 = *(LAS v4u*)(ub + 2 * U_UNIT), x1 = *(LAS v4u*)(ub + 3 * U_UNIT);
    const v4u gg = *(LAS v4u*)(sm + G_OFF + T0 * 4 + b * 16);
    u32* o = out32 + (u32)((T0 + b) * 256 + p);
#pragma unroll
    for (int e = 0; e < 4; e++) {
        const float fya0 = (e & 1) ? bfhi(ya0[e >> 1]) : bflo(ya0[e >> 1]), fyb0 = (e & 1) ? bfhi(yb0[e >> 1]) : bflo(yb0[e >> 1]);
        const float fya1 = (e & 1) ? bfhi(ya1[e >> 1]) : bflo(ya1[e >> 1]), fyb1 = (e & 1) ? bfhi(yb1[e >> 1]) : bflo(yb1[e >> 1]);
        const float fkv0 = bfhi(kv0[e]), fx0 = bfhi(x0[e]), fkv1 = bfhi(kv1[e]), fx1 = bfhi(x1[e]);
        const float g = bflo(gg[e]), g1 = bfhi(gg[e]);
        const float a0 = fx0 * (fya0 + fkv0 * dA0), b0 = fyb0 + fx0 * dS0;
        const float a1 = fx1 * (fya1 + fkv1 * dA1), b1 = fyb1 + fx1 * dS1;
        o[e * 512] = pk(g * a0 + g1 * b0, g * a1 + g1 * b1);
    }
}

__global__ void __launch_bounds__(NTHREADS, 3)
fused_kernel(const u16* __restrict__ X, const u16* __restrict__ Kin, const u16* __restrict__ V, const u16* __restrict__ shiftk,
             const u16* __restrict__ hA, const u16* __restrict__ ssmD, const u16* __restrict__ Are, const u16* __restrict__ Aim,
             const u16* __restrict__ Bre, const u16* __restrict__ Bim, const u16* __restrict__ Cre, const u16* __restrict__ Cim,
             const u16* __restrict__ logdt, const u16* __restrict__ Dskip, const u16* __restrict__ gatew,
             char* __restrict__ ws, u32* __restrict__ out32) {
    __shared__ __attribute__((aligned(16))) char smem_[LDS_BYTES];
    const lptr sm = (lptr)smem_;
    const int tid = threadIdx.x;
    const int bid = blockIdx.x;
    const int lane = tid & 63;
    const int w = __builtin_amdgcn_readfirstlane(tid >> 6);
    const int dg = bid & 7, tg = bid >> 3;
    const int p = dg * 32 + tg;
    STAMP(0);
    const lptr cnt = sm + F_OFF;
    u32 pf = 0;
    if (w == 4 || w == 5) {
        u64 pc;
        asm volatile("s_getpc_b64 %0" : "=s"(pc));
        const u32 off = min((u32)(tid - 256) * 128u, (u32)CODE_BYTES - 4u);
        asm volatile("global_load_dword %0, %1, %2" : "=v"(pf) : "v"(off), "s"(pc & ~127ull) : "memory");
    }
    if (tid < 8) *(LAS u32*)(cnt + tid * 4) = 0;
    asm volatile("" :: "s"(X), "s"(Kin), "s"(V), "s"(shiftk), "s"(hA), "s"(ssmD), "s"(Are), "s"(Aim), "s"(Bre), "s"(Bim), "s"(Cre), "s"(Cim),
                 "s"(logdt), "s"(Dskip), "s"(gatew), "s"(ws), "s"(out32));
    RAW_BARRIER();

    if (w < 4) {
        const int cp = tid & 31, rb = tid >> 5;
        const int c = dg * 64 + cp * 2;
        u32 kk[11], vv[8], xx[8];
        {
            const __amdgpu_buffer_rsrc_t k_rsrc = __builtin_amdgcn_make_buffer_rsrc((void*)Kin, 0, S_ * D_ * 2, 0x00020000);
            const u32 o = (u32)((tg * 64 + rb * 8) * D_ + c) * 2u;
            kk[0] = __builtin_amdgcn_raw_buffer_load_b32(k_rsrc, o - 3072u, 0, 0);
            kk[1] = __builtin_amdgcn_raw_buffer_load_b32(k_rsrc, o - 2048u, 0, 0);
            kk[2] = __builtin_amdgcn_raw_buffer_load_b32(k_rsrc, o - 1024u, 0, 0);
#pragma unroll
            for (int j = 0; j < 8; j++) kk[3 + j] = ldg<u32>(Kin, o + j * 1024);
#pragma unroll
            for (int j = 0; j < 8; j++) vv[j] = ldg<u32>(V, o + j * 1024);
#pragma unroll
            for (int j = 0; j < 8; j++) xx[j] = ldg<u32>(X, o + j * 1024);
        }
        const v4u sk = ldg<v4u>(shiftk, (u32)c * 8u);
        __builtin_amdgcn_sched_barrier(0);
        STAMP(1);
        const int tgate = bid * 8 + w * 2;
        v4u gx0, gx1, gw;
        u64 E;
        {
            float s0[4] = {bflo(sk[0]), bfhi(sk[0]), bflo(sk[1]), bfhi(sk[1])};
            float s1[4] = {bflo(sk[2]), bfhi(sk[2]), bflo(sk[3]), bfhi(sk[3])};
            u32 kvp[8];
#pragma unroll
            for (int j = 0; j < 8; j++) {
                float a0 = bflo(kk[j]) * s0[3] + bflo(kk[j + 1]) * s0[2] + bflo(kk[j + 2]) * s0[1] + bflo(kk[j + 3]) * s0[0];
                float a1 = bfhi(kk[j]) * s1[3] + bfhi(kk[j + 1]) * s1[2] + bfhi(kk[j + 2]) * s1[1] + bfhi(kk[j + 3]) * s1[0];
                kvp[j] = pk(a0 * bflo(vv[j]), a1 * bfhi(vv[j]));
            }
            __builtin_amdgcn_sched_barrier(0);
            STAMP(2);
            gx0 = ldg<v4u>(X, (u32)(tgate * D_ + lane * 8) * 2u); gx1 = ldg<v4u>(X, (u32)((tgate + 1) * D_ + lane * 8) * 2u);
            gw = ldg<v4u>(gatew, (u32)lane * 16u);
            E = __hip_atomic_load((GAS u64*)(ws + WS_E), __ATOMIC_RELAXED, __HIP_MEMORY_SCOPE_AGENT);
            __builtin_amdgcn_sched_barrier(0);
            const lptr base = sm + TILE_OFF + (cp * 2) * 128 + ((rb ^ (cp & 7)) * 16);
            v4u a, b;
#pragma unroll
            for (int j = 0; j < 4; j++) {
                a[j] = __builtin_amdgcn_perm(kvp[2 * j + 1], kvp[2 * j], 0x05040100);
                b[j] = __builtin_amdgcn_perm(kvp[2 * j + 1], kvp[2 * j], 0x07060302);
            }
            *(LAS v4u*)(base) = a; *(LAS v4u*)(base + 128) = b;
#pragma unroll
            for (int j = 0; j < 4; j++) {
                a[j] = __builtin_amdgcn_perm(xx[2 * j + 1], xx[2 * j], 0x05040100);
                b[j] = __builtin_amdgcn_perm(xx[2 * j + 1], xx[2 * j], 0x07060302);
            }
            *(LAS v4u*)(base + 8192) = a; *(LAS v4u*)(base + 8192 + 128) = b;
        }
        lds_arrive(cnt + 12);
        wait_lds(cnt + 12, 4);
        const __amdgpu_buffer_rsrc_t ut_rsrc = __builtin_amdgcn_make_buffer_rsrc(ws + WS_UT, 0, 2 * D_ * S_ * 2, 0x00020000);
        {
            v4u d[4];
#pragma unroll
            for (int i = 0; i < 4; i++) {
                const int ch = (i & 1) * 32 + (tid >> 3), ck = tid & 7;
                d[i] = *(LAS v4u*)(sm + TILE_OFF + (i >> 1) * 8192 + ch * 128 + ((ck ^ ((ch >> 1) & 7)) * 16));
            }
#pragma unroll
            for (int i = 0; i < 4; i++) {
                const int ch = (i & 1) * 32 + (tid >> 3), ck = tid & 7;
                __builtin_amdgcn_raw_buffer_store_b128(d[i], ut_rsrc, (u32)((((i >> 1) * D_ + dg * 64 + ch) * S_ + tg * 64 + ck * 8) * 2), 0, 16);
            }
        }
        STAMP(3);
        const u64 epoch = (u64)__builtin_amdgcn_readfirstlane((u32)E) + ((u64)__builtin_amdgcn_readfirstlane((u32)(E >> 32)) << 32) + 1ull;
        u32 gb;
        {
            float s0 = 0.f, s1 = 0.f;
#pragma unroll
            for (int j = 0; j < 4; j++) {
                u32 p0 = pk(bflo(gx0[j]) * bflo(gw[j]), bfhi(gx0[j]) * bfhi(gw[j]));
                u32 p1 = pk(bflo(gx1[j]) * bflo(gw[j]), bfhi(gx1[j]) * bfhi(gw[j]));
                s0 += bflo(p0) + bfhi(p0);
                s1 += bflo(p1) + bfhi(p1);
            }
            s0 = dpp_add<0x111>(s0); s1 = dpp_add<0x111>(s1);
            s0 = dpp_add<0x112>(s0); s1 = dpp_add<0x112>(s1);
            s0 = dpp_add<0x114>(s0); s1 = dpp_add<0x114>(s1);
            s0 = dpp_add<0x118>(s0); s1 = dpp_add<0x118>(s1);
            const int i0 = __float_as_int(s0), i1 = __float_as_int(s1);
            const float t0_ = __int_as_float(__builtin_amdgcn_readlane(i0, 15)) + __int_as_float(__builtin_amdgcn_readlane(i0, 31)) +
                              __int_as_float(__builtin_amdgcn_readlane(i0, 47)) + __int_as_float(__builtin_amdgcn_readlane(i0, 63));
            const float t1_ = __int_as_float(__builtin_amdgcn_readlane(i1, 15)) + __int_as_float(__builtin_amdgcn_readlane(i1, 31)) +
                              __int_as_float(__builtin_amdgcn_readlane(i1, 47)) + __int_as_float(__builtin_amdgcn_readlane(i1, 63));
            const float z = rbf((lane & 1) ? t1_ : t0_);
            const float g = 1.f / (1.f + __expf(-z));
            gb = pk(g, g) & 0xffff;
        }
        asm volatile("s_waitcnt vmcnt(0)" ::: "memory");
        lds_arrive(cnt + 16);
        STAMP(4);
        if (w == 0) {
            wait_lds(cnt + 16, 4);
            if (lane < 32) {
                GAS u64* inbox = (GAS u64*)(ws + WS_INBOX) + (dg * 32 + lane) * 32 + tg;
                __hip_atomic_store(inbox, epoch, __ATOMIC_RELAXED, __HIP_MEMORY_SCOPE_AGENT);
            }
        }
        if (lane < 2) __hip_atomic_store((GAS u64*)(ws + WS_GATE) + tgate + lane, (epoch << 16) | gb, __ATOMIC_RELAXED, __HIP_MEMORY_SCOPE_AGENT);
        STAMP(5);
        {
            const int pidx = w * 8 - 1 + min(lane, 8);
            GAS u64* inbox = (GAS u64*)(ws + WS_INBOX) + p * 32 + max(pidx, 0);
#pragma unroll 1
            for (u32 spins = 0; spins < SPIN_CAP; spins++) {
                const u64 v = __hip_atomic_load(inbox, __ATOMIC_RELAXED, __HIP_MEMORY_SCOPE_AGENT);
                if (__all(v == epoch)) break;
                __builtin_amdgcn_s_sleep(1);
            }
            asm volatile("" ::: "memory");
            STAMP(6);
            v4u ud[4];
            u32 up[4];
            const int t = w * 512 + lane * 8;
#pragma unroll
            for (int u = 0; u < 4; u++) {
                const u32 off = (u32)((((u & 1) * D_ + p * 2 + (u >> 1)) * S_ + t) * 2);
                ud[u] = __builtin_amdgcn_raw_buffer_load_b128(ut_rsrc, off, 0, 16);
                up[u] = __builtin_amdgcn_raw_buffer_load_b32(ut_rsrc, t ? off - 4u : off, 0, 16);
            }
            wait_lds(cnt + 24, 8);
            *(LAS v4u*)(sm + U_OFF + (tid >> 5) * U_COPY + (tid & 31) * 16) = (v4u){0, 0, 0, 0};
#pragma unroll
            for (int u = 0; u < 4; u++) {
                const lptr dst = sm + U_OFF + u * U_UNIT + 512 + t * 2;
                const u32 prev = t ? up[u] : 0u;
                *(LAS v4u*)(dst + U_COPY) = ud[u];
                *(LAS v4u*)(dst) = (v4u){__builtin_amdgcn_alignbit(ud[u][0], prev, 16), __builtin_amdgcn_alignbit(ud[u][1], ud[u][0], 16),
                                         __builtin_amdgcn_alignbit(ud[u][2], ud[u][1], 16), __builtin_amdgcn_alignbit(ud[u][3], ud[u][2], 16)};
            }
        }
        STAMP(7);
        RAW_BARRIER();
        STAMP(8);
        const int g4 = lane >> 4, n = lane & 15;
        const lptr ha = sm + w * H_UNIT + (n & 3) * H_COPY + 48 - 16 * (n >> 2) + 16 * g4;
        const lptr ub = sm + U_OFF + w * U_UNIT + (n & 1) * U_COPY + 512 + 64 * (n >> 1) + 16 * g4 - 7 * 64;
        const lptr yb = sm + Y_OFF + w * 4096 + 64 * (n >> 1) + 16 * g4 + 8 * (n & 1);
        v8bf A0[8], B0[8], A1[8], B1[8];
        v4f acc[8];
#pragma unroll
        for (int i = 0; i < 8; i++) acc[i] = (v4f){0.f, 0.f, 0.f, 0.f};
        load_set<0>(A0, B0, ha, ub);
        conv_steps<0>(A0, B0, A1, B1, acc, ha, ub, yb, cnt
        );
        STAMP_FLUSH();
    } else {
        const int kt = tid - 256;
        const int half = __builtin_amdgcn_readfirstlane(kt >> 8);
        const int kch = (kt >> 7) & 1, kn = kt & 127;
        const int kd = p * 2 + kch;
        const u32 pi = (u32)(kd * N_ + kn) * 2u;
        const u16 p_ldt = ldg<u16>(logdt, (u32)kd * 2u);
        const u16 p_are = ldg<u16>(Are, pi), p_aim = ldg<u16>(Aim, pi), p_bre = ldg<u16>(Bre, pi), p_bim = ldg<u16>(Bim, pi), p_cre = ldg<u16>(Cre, pi), p_cim = ldg<u16>(Cim, pi);
        const int hck = kt & 255;
        const u32 ho = (u32)((p * 2 + half) * S_ + hck * 8) * 2u;
        v4u hd;
        v3u hm;
        u64 E;
        const u32 dAp = *(const u32*)(ssmD + p * 2), dSp = *(const u32*)(Dskip + p * 2);
        __builtin_amdgcn_sched_barrier(0);
        STAMP(1);
        u32 lv[14];
#pragma unroll
        for (int j = 0; j < 14; j++) {
            const int l = hck * 8 - 6 + j;
            lv[j] = (u32)rbf((float)(j < 6 ? max(l, 0) : l)) * 2;
        }
        {
            const float dt = rbf(expf(bf1(p_ldt)));
            const float a = dt * bf1(p_are), b = dt * bf1(p_aim);
            const float br = dt * bf1(p_bre), bi = dt * bf1(p_bim);
            const float cre = bf1(p_cre), cim = bf1(p_cim);
            v2f CB;
            CB[0] = cre * br - cim * bi;
            CB[1] = -(cre * bi + cim * br);
            const float a2 = a * 1.4426950408889634f;
            const float rh = b * 0.15915494309189535f;
            const float rl = __builtin_fmaf(b, 0.15915494309189535f, -rh) + b * 6.4206383e-9f;
            const lptr tab = sm + R2_OFF + kch * TAB_CH + kn * 4;
            asm volatile("" :: "v"(a2), "v"(rh), "v"(rl), "v"(CB), "v"(pf));
            __builtin_amdgcn_sched_barrier(0);
            STAMP(2);
            hd = ldg<v4u>(hA, ho);
            hm = ldg<v3u>(hA, ho - (hck ? 12u : 0u));
            E = __hip_atomic_load((GAS u64*)(ws + WS_E), __ATOMIC_RELAXED, __HIP_MEMORY_SCOPE_AGENT);
            __builtin_amdgcn_sched_barrier(0);
#define PUT(row, v) (*(LAS u32*)(tab + (row) * TAB_PITCH) = pk2(v))
#define PUTC(row, v) (*(LAS u32*)(tab + (row) * TAB_PITCH) = pk((v)[0], -(v)[1]))
            v2f t0_, t1_;
            if (half == 0) {
                v2f E0 = epow<true>(a2, rh, rl, 1.f), E3 = epow<true>(a2, rh, rl, 8.f), E7 = epow<true>(a2, rh, rl, 128.f);
                v2f E1, E2, F[8], F8;
                csqr2(E1, E0, F[2], E3);
                csqr2(E2, E1, F[4], F[2]);
                v2f P[8];
                P[0] = CB; F[1] = E3;
                cmul2(P[1], P[0], E0, P[2], P[0], E1);
                cmul2(P[3], P[1], E1, P[4], P[0], E2);
                cmul2(P[5], P[1], E2, P[6], P[2], E2);
                cmul2(P[7], P[3], E2, F8, F[4], F[4]);
#pragma unroll
                for (int e = 0; e < 8; e += 2) {
                    PUT(e, P[e]); PUT(e + 1, P[e + 1]);
                    cmul2(t0_, P[e], E7, t1_, P[e + 1], E7);
                    PUT(8 + e, t0_); PUT(9 + e, t1_);
                }
                F[0][0] = 1.f; F[0][1] = 0.f;
                cmul2(F[3], F[1], F[2], F[5], F[4], F[1]);
                cmul2(F[6], F[4], F[2], t0_, F8, F[1]);
                PUTC(48 + 9, t0_);
                cmul2(F[7], F[4], F[3], t0_, F8, F[2]);
                PUTC(48 + 10, t0_);
#pragma unroll
                for (int e = 0; e < 8; e++) PUTC(48 + e, F[e]);
                PUTC(48 + 8, F8);
                cmul2(t0_, F8, F[3], t1_, F8, F[4]); PUTC(48 + 11, t0_); PUTC(48 + 12, t1_);
                cmul2(t0_, F8, F[5], t1_, F8, F[6]); PUTC(48 + 13, t0_); PUTC(48 + 14, t1_);
                cmul2(t0_, F8, F[7], t1_, F8, F[7]); PUTC(48 + 15, t0_);
            } else {
                v2f E1 = epow<true>(a2, rh, rl, 2.f), E7 = epow<true>(a2, rh, rl, 128.f), E9 = epow<true>(a2, rh, rl, 512.f);
                v2f E2, E8, E10, E11, P2, P4, P6, W3, R[4], Q0, Q1, T0, T1, T2, T3;
                csqr2(E2, E1, E8, E7);
                cmul2(E10, E9, E9, P2, CB, E1);
                cmul2(E11, E10, E10, P4, CB, E2);
                cmul2(P6, P2, E2, W3, E7, E8);
                cmul2(R[0], CB, E8, R[1], P2, E8);
                cmul2(R[2], P4, E8, R[3], P6, E8);
#pragma unroll
                for (int e = 0; e < 4; e += 2) {
                    PUT(16 + e, R[e]); PUT(17 + e, R[e + 1]);
                    cmul2(t0_, R[e], E7, t1_, R[e + 1], E7);
                    PUT(20 + e, t0_); PUT(21 + e, t1_);
                }
                cmul2(Q0, CB, E9, Q1, P4, E9);
                PUT(24, Q0); PUT(28, Q1);
                cmul2(t0_, Q0, E7, t1_, Q1, E7); PUT(25, t0_); PUT(29, t1_);
                cmul2(t0_, Q0, E8, t1_, Q1, E8); PUT(26, t0_); PUT(30, t1_);
                cmul2(t0_, Q0, W3, t1_, Q1, W3); PUT(27, t0_); PUT(31, t1_);
                cmul2(T0, CB, E10, t0_, CB, E11); PUT(32, T0); PUT(40, t0_);
                cmul2(T1, T0, E7, T2, T0, E8); PUT(33, T1); PUT(34, T2);
                cmul2(T3, T0, W3, t0_, T0, E9); PUT(35, T3); PUT(36, t0_);
                cmul2(t0_, T1, E9, t1_, T2, E9); PUT(37, t0_); PUT(38, t1_);
                cmul2(t0_, T3, E9, t1_, T3, E9); PUT(39, t0_);
            }
        }
        lds_arrive(cnt + 20);
        STAMP(3);
        {
            if (hck == 0) { hm[0] = 0; hm[1] = 0; hm[2] = 0; }
            u32 r[7];
            r[0] = rot16(hd[3]); r[1] = rot16(hd[2]); r[2] = rot16(hd[1]); r[3] = rot16(hd[0]);
            r[4] = rot16(hm[2]); r[5] = rot16(hm[1]); r[6] = rot16(hm[0]);
            const lptr dst = sm + (half * 2) * H_UNIT + (255 - hck) * 16;
            *(LAS v4u*)(dst) = (v4u){r[3], r[4], r[5], r[6]};
            *(LAS v4u*)(dst + H_COPY) = (v4u){r[2], r[3], r[4], r[5]};
            *(LAS v4u*)(dst + 2 * H_COPY) = (v4u){r[1], r[2], r[3], r[4]};
            *(LAS v4u*)(dst + 3 * H_COPY) = (v4u){r[0], r[1], r[2], r[3]};
        }
        if (kt < 48) {
            const int uc = kt / 3, ck = kt - uc * 3;
            *(LAS v4u*)(sm + uc * H_COPY + (256 + ck) * 16) = (v4u){0, 0, 0, 0};
        }
        const int id = w - 4;
        if (id < 6) {
            const int c2 = (id >= 3) ? 1 : 0, mt = id - c2 * 3;
            const int g4 = lane >> 4;
            int base, step;
            if (mt == 0) { base = ((g4 >> 1) << 7) + ((g4 & 1) << 2); step = 1; }
            else if (mt == 1) { base = (g4 < 2) ? (256 + (g4 << 7)) : (512 + ((g4 & 1) << 2)); step = (g4 < 2) ? 2 : 128; }
            else { base = (g4 < 2) ? (1024 + (g4 << 9)) : (2048 + ((g4 & 1) << 3)); step = 128; }
            const lptr T = sm + T_OFF + c2 * T_CH + (base + 8 * (lane & 15)) * 2;
            const lptr ga = sm + R2_OFF + c2 * TAB_CH + (mt * 16 + (lane & 15)) * TAB_PITCH + (lane >> 4) * 16;
            const lptr fb = sm + R2_OFF + c2 * TAB_CH + (48 + (lane & 15)) * TAB_PITCH + (lane >> 4) * 16;
            wait_lds(cnt + 20, 8);
            STAMP(4);
            v8bf A[8], B[8];
#pragma unroll
            for (int s = 0; s < 8; s++) { A[s] = *(LAS v8bf*)(ga + s * 64); B[s] = *(LAS v8bf*)(fb + s * 64); }
            v4f acc0 = {0.f, 0.f, 0.f, 0.f}, acc1 = {0.f, 0.f, 0.f, 0.f};
#pragma unroll
            for (int s = 0; s < 8; s += 2) {
                acc0 = __builtin_amdgcn_mfma_f32_16x16x32_bf16(A[s], B[s], acc0, 0, 0, 0);
                acc1 = __builtin_amdgcn_mfma_f32_16x16x32_bf16(A[s + 1], B[s + 1], acc1, 0, 0, 0);
            }
            const u32 o01 = pk(acc0[0] + acc1[0], acc0[1] + acc1[1]), o23 = pk(acc0[2] + acc1[2], acc0[3] + acc1[3]);
            *(LAS u16*)(T) = (u16)o01;
            *(LAS u16*)(T + step * 2) = (u16)(o01 >> 16);
            *(LAS u16*)(T + step * 4) = (u16)o23;
            *(LAS u16*)(T + step * 6) = (u16)(o23 >> 16);
        }
        lds_arrive(cnt + 24);
        wait_lds(cnt + 24, 8);
        STAMP(5);
        {
            const lptr T = sm + T_OFF + half * T_CH;
            u32 hv[14];
#pragma unroll
            for (int j = 0; j < 14; j++) {
                const u32 x = *(LAS u16*)(T + lv[j]);
                hv[j] = (j < 6 && hck == 0) ? 0u : x;
            }
            u32 r[7];
#pragma unroll
            for (int i = 0; i < 7; i++) r[i] = hv[13 - 2 * i] | (hv[12 - 2 * i] << 16);
            const lptr dst = sm + (half * 2 + 1) * H_UNIT + (255 - hck) * 16;
            *(LAS v4u*)(dst) = (v4u){r[3], r[4], r[5], r[6]};
            *(LAS v4u*)(dst + H_COPY) = (v4u){r[2], r[3], r[4], r[5]};
            *(LAS v4u*)(dst + 2 * H_COPY) = (v4u){r[1], r[2], r[3], r[4]};
            *(LAS v4u*)(dst + 3 * H_COPY) = (v4u){r[0], r[1], r[2], r[3]};
        }
        STAMP(6);
        const u64 epoch = (u64)__builtin_amdgcn_readfirstlane((u32)E) + ((u64)__builtin_amdgcn_readfirstlane((u32)(E >> 32)) << 32) + 1ull;
        const float dA0 = bflo(dAp), dA1 = bfhi(dAp), dS0 = bflo(dSp), dS1 = bfhi(dSp);
        RAW_BARRIER();
        STAMP(8);
        {
            GAS u64* gr = (GAS u64*)(ws + WS_GATE) + kt;
            const u64 tag = epoch & 0xFFFFFFFFFFFFull;
            u32 gv[4];
#pragma unroll 1
            for (u32 spins = 0; spins < SPIN_CAP; spins++) {
                bool ok = true;
#pragma unroll
                for (int i = 0; i < 4; i++) {
                    const u64 v = __hip_atomic_load(gr + i * 512, __ATOMIC_RELAXED, __HIP_MEMORY_SCOPE_AGENT);
                    gv[i] = (u32)v & 0xffff;
                    ok &= (v >> 16) == tag;
                }
                if (__all(ok)) break;
                __builtin_amdgcn_s_sleep(1);
            }
#pragma unroll
            for (int i = 0; i < 4; i++) {
                const int t = kt + i * 512;
                *(LAS u32*)(sm + G_OFF + (t >> 3) * 32 + (t & 1) * 16 + ((t >> 1) & 3) * 4) = gv[i] | (pk(0.f, 1.f - bf1((u16)gv[i])) & 0xffff0000u);
            }
        }
        lds_arrive(cnt + 28);
        wait_lds(cnt + 28, 8);
        if (bid == 0 && tid == 256) __hip_atomic_store((GAS u64*)(ws + WS_E), epoch, __ATOMIC_RELAXED, __HIP_MEMORY_SCOPE_AGENT);
        STAMP(7);
        wait_lds(cnt, 4);
        STAMP(9);
        if (kt < PASS_SPLIT * 64) epi_quad(sm, (kt >> 1) * 8, kt & 1, out32, p, dA0, dA1, dS0, dS1);
        const int tb = 511 - kt;
        if (w >= 12 - ((8 - PASS_SPLIT) * 64 + 63) / 64) {
            wait_lds(cnt + 4, 4);
            STAMP(10);
            if (tb < (8 - PASS_SPLIT) * 64) epi_quad(sm, PASS_SPLIT * 256 + (tb >> 1) * 8, tb & 1, out32, p, dA0, dA1, dS0, dS1);
        }
        STAMP(12);
        STAMP_FLUSH();
    }
}

extern "C" void kernel_launch(void* const* d_in, const int* in_sizes, int n_in,
                              void* d_out, int out_size, void* d_ws, size_t ws_size,
                              hipStream_t stream) {
    fused_kernel<<<256, NTHREADS, 0, stream>>>((const u16*)d_in[0], (const u16*)d_in[1], (const u16*)d_in[2], (const u16*)d_in[3],
                                          (const u16*)d_in[4], (const u16*)d_in[5], (const u16*)d_in[7], (const u16*)d_in[8],
                                          (const u16*)d_in[9], (const u16*)d_in[10], (const u16*)d_in[11], (const u16*)d_in[12],
                                          (const u16*)d_in[13], (const u16*)d_in[14], (const u16*)d_in[15], (char*)d_ws, (u32*)d_out);
}
```

```cpp
#include <hip/hip_runtime.h>
#include <stdint.h>

typedef unsigned short u16;
typedef unsigned int u32;
typedef unsigned long long u64;
typedef short v4s __attribute__((ext_vector_type(4)));
typedef short v8s __attribute__((ext_vector_type(8)));
typedef __bf16 v8bf __attribute__((ext_vector_type(8)));
typedef __bf16 v2bf __attribute__((ext_vector_type(2)));
typedef float v4f __attribute__((ext_vector_type(4)));
typedef float v2f __attribute__((ext_vector_type(2)));
typedef u32 v4u __attribute__((ext_vector_type(4)));
typedef u32 v2u __attribute__((ext_vector_type(2)));
typedef u32 v3u __attribute__((ext_vector_type(3)));
#define LAS __attribute__((address_space(3)))
#define GAS __attribute__((address_space(1)))
typedef LAS char* lptr;

#define S_ 2048
#define D_ 512
#define N_ 128

#define STAMP(i) do { } while (0)
#define STAMP_FLUSH() do { } while (0)

__device__ __forceinline__ float bflo(u32 v) { return __uint_as_float(v << 16); }
__device__ __forceinline__ float bfhi(u32 v) { return __uint_as_float(v & 0xffff0000u); }
__device__ __forceinline__ float bf1(u16 v) { return __uint_as_float(((u32)v) << 16); }
__device__ __forceinline__ u32 pk(float lo, float hi) {
    v2f x = {lo, hi};
    return __builtin_bit_cast(u32, __builtin_convertvector(x, v2bf));
}
__device__ __forceinline__ u32 pk2(v2f x) { return __builtin_bit_cast(u32, __builtin_convertvector(x, v2bf)); }
__device__ __forceinline__ u32 rot16(u32 v) { return __builtin_amdgcn_alignbit(v, v, 16); }
__device__ __forceinline__ float rbf(float f) { return bflo(pk(f, f)); }

template <typename T>
__device__ __forceinline__ T ldg(const void* base, u32 off) { return *(const GAS T*)((const GAS char*)base + off); }

__device__ __forceinline__ void cmul2(v2f& c0, v2f a0, v2f b0, v2f& c1, v2f a1, v2f b1) {
    asm("v_pk_mul_f32 %0, %2, %3 op_sel:[0,0] op_sel_hi:[1,0]\n\t"
        "v_pk_mul_f32 %1, %4, %5 op_sel:[0,0] op_sel_hi:[1,0]\n\t"
        "v_pk_fma_f32 %0, %2, %3, %0 op_sel:[1,1,0] op_sel_hi:[0,1,1] neg_lo:[1,0,0]\n\t"
        "v_pk_fma_f32 %1, %4, %5, %1 op_sel:[1,1,0] op_sel_hi:[0,1,1] neg_lo:[1,0,0]"
        : "=&v"(c0), "=&v"(c1) : "v"(a0), "v"(b0), "v"(a1), "v"(b1));
}

__device__ __forceinline__ void csqr2(v2f& c0, v2f a0, v2f& c1, v2f a1) { cmul2(c0, a0, a0, c1, a1, a1); }

template <bool CONJ>
__device__ __forceinline__ v2f epow(float a2, float rh, float rl, float s) {
    float e = __builtin_amdgcn_exp2f(a2 * s);
    float x = __builtin_fmaf(rl, s, __builtin_amdgcn_fractf(rh * s));
    float cs = __builtin_amdgcn_cosf(x), sn = __builtin_amdgcn_sinf(x);
    v2f c;
    c[0] = e * cs;
    c[1] = CONJ ? -(e * sn) : (e * sn);
    return c;
}

#define H_COPY 4160
#define H_UNIT (4 * H_COPY)
#define R2_OFF (4 * H_UNIT)
#define TAB_PITCH 544
#define TAB_CH (64 * TAB_PITCH)
#define U_OFF R2_OFF
#define U_COPY 4640
#define U_UNIT (2 * U_COPY)
#define Y_OFF (U_OFF + 4 * U_UNIT)
#define T_OFF (R2_OFF + 2 * TAB_CH)
#define T_CH 5248
#define TILE_OFF (T_OFF + 2 * T_CH)
#define F_OFF (TILE_OFF + 16384)
#define LDS_BYTES (F_OFF + 32)

#define WS_E 0
#define WS_INBOX 4096
#define WS_GATE (128 * 1024)
#define WS_UT (1024 * 1024)
#define WS_STAMPS (8 * 1024 * 1024)

#define SPIN_CAP (1u << 20)
#define RAW_BARRIER() do { asm volatile("s_waitcnt lgkmcnt(0)" ::: "memory"); __builtin_amdgcn_s_barrier(); asm volatile("" ::: "memory"); } while (0)

template <int CTRL>
__device__ __forceinline__ float dpp_add(float v) {
    int t = __builtin_amdgcn_update_dpp(0, __float_as_int(v), CTRL, 0xf, 0xf, true);
    return v + __int_as_float(t);
}

__device__ __forceinline__ void lds_arrive(lptr word) {
    asm volatile("s_waitcnt lgkmcnt(0)" ::: "memory");
    if ((threadIdx.x & 63) == 0) __hip_atomic_fetch_add((LAS u32*)word, 1u, __ATOMIC_RELAXED, __HIP_MEMORY_SCOPE_WORKGROUP);
}
__device__ __forceinline__ void wait_lds(lptr word, u32 want) {
#pragma unroll 1
    for (u32 spins = 0; spins < SPIN_CAP; spins++) {
        const u32 v = __hip_atomic_load((LAS u32*)word, __ATOMIC_RELAXED, __HIP_MEMORY_SCOPE_WORKGROUP);
        if (__builtin_amdgcn_readfirstlane(v) >= want) break;
        __builtin_amdgcn_s_sleep(1);
    }
    asm volatile("" ::: "memory");
}

#define NSTEP 16
#define PASS_SPLIT 5
__host__ __device__ constexpr int pass_lo(int p) { return p == 0 ? 0 : PASS_SPLIT; }
__host__ __device__ constexpr int pass_hi(int p) { return p == 0 ? PASS_SPLIT - 1 : 7; }
__host__ __device__ constexpr int pass_nm(int p) {
    int c = 0;
    for (int i = 0; i <= pass_hi(p); i++) for (int q = 0; q <= pass_hi(p); q++) if (i + q >= pass_lo(p) && i + q <= pass_hi(p)) c++;
    return c;
}

template <int S>
__device__ __forceinline__ void load_set(v8bf (&A)[8], v8bf (&B)[8], lptr ha, lptr ub) {
    constexpr int P = S / 8, r = S % 8, N = pass_hi(P) + 1;
#pragma unroll
    for (int i = 0; i < N; i++) {
        A[i] = *(LAS v8bf*)(ha + (7 - r) * 64 + (7 - i) * 512);
        B[i] = *(LAS v8bf*)(ub + (7 - r) * 64 + i * 512);
    }
}
template <int S>
__device__ __forceinline__ void mma_set(const v8bf (&A)[8], const v8bf (&B)[8], v4f (&acc)[8]) {
    constexpr int P = S / 8, LO = pass_lo(P), HI = pass_hi(P);
#pragma unroll
    for (int i = 0; i <= HI; i++)
#pragma unroll
        for (int q = 0; q <= HI; q++)
            if (i + q >= LO && i + q <= HI)
                acc[i + q] = __builtin_amdgcn_mfma_f32_16x16x32_bf16(A[i], B[q], acc[i + q], 0, 0, 0);
}
template <int S>
__device__ __forceinline__ void sched_step() {
    constexpr int M = pass_nm(S / 8);
    constexpr int R = (S + 1 < NSTEP) ? 2 * (pass_hi((S + 1) / 8) + 1) : 0;
    constexpr int per = (R + M - 1) / M;
#pragma unroll
    for (int k = 0; k < M; k++) {
        __builtin_amdgcn_sched_group_barrier(0x8, 1, 0);
        if (R > 0) __builtin_amdgcn_sched_group_barrier(0x100, per, 0);
    }
}
template <int S>
__device__ __forceinline__ void conv_steps(v8bf (&A0)[8], v8bf (&B0)[8], v8bf (&A1)[8], v8bf (&B1)[8], v4f (&acc)[8], lptr ha, lptr ub, lptr yb, lptr flags
) {
    if constexpr (S < NSTEP) {
        if constexpr (S + 1 < NSTEP) {
            if constexpr (S & 1) load_set<S + 1>(A0, B0, ha, ub); else load_set<S + 1>(A1, B1, ha, ub);
        }
        if constexpr (S & 1) mma_set<S>(A1, B1, acc); else mma_set<S>(A0, B0, acc);
        sched_step<S>();
        if constexpr (S % 8 == 7) {
            constexpr int P = S / 8;
#pragma unroll
            for (int t = pass_lo(P); t <= pass_hi(P); t++) {
                v2u o = {pk(acc[t][0], acc[t][1]), pk(acc[t][2], acc[t][3])};
                *(LAS v2u*)(yb + t * 512) = o;
            }
            lds_arrive(flags + P * 4);
            STAMP(9 + P);
        }
        conv_steps<S + 1>(A0, B0, A1, B1, acc, ha, ub, yb, flags
        );
    }
}

#define NTHREADS 768
#define CODE_BYTES 15360
#define G_OFF TILE_OFF

__device__ __forceinline__ void epi_quad(lptr sm, int T0, int b, u32* __restrict__ out32, int p, float dA0, float dA1, float dS0, float dS1) {
    const lptr yp = sm + Y_OFF + T0 * 2 + b * 8;
    const v2u ya0 = *(LAS v2u*)(yp), yb0 = *(LAS v2u*)(yp + 4096), ya1 = *(LAS v2u*)(yp + 8192), yb1 = *(LAS v2u*)(yp + 12288);
    const lptr ub = sm + U_OFF + b * U_COPY + 512 + T0 * 2;
    const v4u kv0 = *(LAS v4u*)(ub), x0 = *(LAS v4u*)(ub + U_UNIT), kv1 = *(LAS v4u*)(ub + 2 * U_UNIT), x1 = *(LAS v4u*)(ub + 3 * U_UNIT);
    const v4u gg = *(LAS v4u*)(sm + G_OFF + T0 * 4 + b * 16);
    u32* o = out32 + (u32)((T0 + b) * 256 + p);
#pragma unroll
    for (int e = 0; e < 4; e++) {
        const float fya0 = (e & 1) ? bfhi(ya0[e >> 1]) : bflo(ya0[e >> 1]), fyb0 = (e & 1) ? bfhi(yb0[e >> 1]) : bflo(yb0[e >> 1]);
        const float fya1 = (e & 1) ? bfhi(ya1[e >> 1]) : bflo(ya1[e >> 1]), fyb1 = (e & 1) ? bfhi(yb1[e >> 1]) : bflo(yb1[e >> 1]);
        const float fkv0 = bfhi(kv0[e]), fx0 = bfhi(x0[e]), fkv1 = bfhi(kv1[e]), fx1 = bfhi(x1[e]);
        const float g = bflo(gg[e]), g1 = bfhi(gg[e]);
        const float a0 = fx0 * (fya0 + fkv0 * dA0), b0 = fyb0 + fx0 * dS0;
        const float a1 = fx1 * (fya1 + fkv1 * dA1), b1 = fyb1 + fx1 * dS1;
        o[e * 512] = pk(g * a0 + g1 * b0, g * a1 + g1 * b1);
    }
}

__global__ void __launch_bounds__(NTHREADS, 3)
fused_kernel(const u16* __restrict__ X, const u16* __restrict__ Kin, const u16* __restrict__ V, const u16* __restrict__ shiftk,
             const u16* __restrict__ hA, const u16* __restrict__ ssmD, const u16* __restrict__ Are, const u16* __restrict__ Aim,
             const u16* __restrict__ Bre, const u16* __restrict__ Bim, const u16* __restrict__ Cre, const u16* __restrict__ Cim,
             const u16* __restrict__ logdt, const u16* __restrict__ Dskip, const u16* __restrict__ gatew,
             char* __restrict__ ws, u32* __restrict__ out32) {
    __shared__ __attribute__((aligned(16))) char smem_[LDS_BYTES];
    const lptr sm = (lptr)smem_;
    const int tid = threadIdx.x;
    const int bid = blockIdx.x;
    const int lane = tid & 63;
    const int w = __builtin_amdgcn_readfirstlane(tid >> 6);
    const int dg = bid & 7, tg = bid >> 3;
    const int p = dg * 32 + tg;
    STAMP(0);
    const lptr cnt = sm + F_OFF;
    u32 pf = 0;
    if (w == 4 || w == 5) {
        u64 pc;
        asm volatile("s_getpc_b64 %0" : "=s"(pc));
        const u32 off = min((u32)(tid - 256) * 128u, (u32)CODE_BYTES - 4u);
        asm volatile("global_load_dword %0, %1, %2" : "=v"(pf) : "v"(off), "s"(pc & ~127ull) : "memory");
    }
    if (tid < 8) *(LAS u32*)(cnt + tid * 4) = 0;
    asm volatile("" :: "s"(X), "s"(Kin), "s"(V), "s"(shiftk), "s"(hA), "s"(ssmD), "s"(Are), "s"(Aim), "s"(Bre), "s"(Bim), "s"(Cre), "s"(Cim),
                 "s"(logdt), "s"(Dskip), "s"(gatew), "s"(ws), "s"(out32));
    RAW_BARRIER();

    if (w < 4) {
        __builtin_amdgcn_s_setprio(2);
        const int cp = tid & 31, rb = tid >> 5;
        const int c = dg * 64 + cp * 2;
        u32 kk[11], vv[8], xx[8];
        {
            const __amdgpu_buffer_rsrc_t k_rsrc = __builtin_amdgcn_make_buffer_rsrc((void*)Kin, 0, S_ * D_ * 2, 0x00020000);
            const u32 o = (u32)((tg * 64 + rb * 8) * D_ + c) * 2u;
            kk[0] = __builtin_amdgcn_raw_buffer_load_b32(k_rsrc, o - 3072u, 0, 0);
            kk[1] = __builtin_amdgcn_raw_buffer_load_b32(k_rsrc, o - 2048u, 0, 0);
            kk[2] = __builtin_amdgcn_raw_buffer_load_b32(k_rsrc, o - 1024u, 0, 0);
#pragma unroll
            for (int j = 0; j < 8; j++) kk[3 + j] = ldg<u32>(Kin, o + j * 1024);
#pragma unroll
            for (int j = 0; j < 8; j++) vv[j] = ldg<u32>(V, o + j * 1024);
#pragma unroll
            for (int j = 0; j < 8; j++) xx[j] = ldg<u32>(X, o + j * 1024);
        }
        const v4u sk = ldg<v4u>(shiftk, (u32)c * 8u);
        __builtin_amdgcn_sched_barrier(0);
        STAMP(1);
        const int tgate = bid * 8 + w * 2;
        v4u gx0, gx1, gw;
        u64 E;
        {
            float s0[4] = {bflo(sk[0]), bfhi(sk[0]), bflo(sk[1]), bfhi(sk[1])};
            float s1[4] = {bflo(sk[2]), bfhi(sk[2]), bflo(sk[3]), bfhi(sk[3])};
            u32 kvp[8];
#pragma unroll
            for (int j = 0; j < 8; j++) {
                float a0 = bflo(kk[j]) * s0[3] + bflo(kk[j + 1]) * s0[2] + bflo(kk[j + 2]) * s0[1] + bflo(kk[j + 3]) * s0[0];
                float a1 = bfhi(kk[j]) * s1[3] + bfhi(kk[j + 1]) * s1[2] + bfhi(kk[j + 2]) * s1[1] + bfhi(kk[j + 3]) * s1[0];
                kvp[j] = pk(a0 * bflo(vv[j]), a1 * bfhi(vv[j]));
            }
            __builtin_amdgcn_sched_barrier(0);
            STAMP(2);
            gx0 = ldg<v4u>(X, (u32)(tgate * D_ + lane * 8) * 2u); gx1 = ldg<v4u>(X, (u32)((tgate + 1) * D_ + lane * 8) * 2u);
            gw = ldg<v4u>(gatew, (u32)lane * 16u);
            E = __hip_atomic_load((GAS u64*)(ws + WS_E), __ATOMIC_RELAXED, __HIP_MEMORY_SCOPE_AGENT);
            __builtin_amdgcn_sched_barrier(0);
            const lptr base = sm + TILE_OFF + (cp * 2) * 128 + ((rb ^ (cp & 7)) * 16);
            v4u a, b;
#pragma unroll
            for (int j = 0; j < 4; j++) {
                a[j] = __builtin_amdgcn_perm(kvp[2 * j + 1], kvp[2 * j], 0x05040100);
                b[j] = __builtin_amdgcn_perm(kvp[2 * j + 1], kvp[2 * j], 0x07060302);
            }
            *(LAS v4u*)(base) = a; *(LAS v4u*)(base + 128) = b;
#pragma unroll
            for (int j = 0; j < 4; j++) {
                a[j] = __builtin_amdgcn_perm(xx[2 * j + 1], xx[2 * j], 0x05040100);
                b[j] = __builtin_amdgcn_perm(xx[2 * j + 1], xx[2 * j], 0x07060302);
            }
            *(LAS v4u*)(base + 8192) = a; *(LAS v4u*)(base + 8192 + 128) = b;
        }
        lds_arrive(cnt + 12);
        wait_lds(cnt + 12, 4);
        const __amdgpu_buffer_rsrc_t ut_rsrc = __builtin_amdgcn_make_buffer_rsrc(ws + WS_UT, 0, 2 * D_ * S_ * 2, 0x00020000);
        {
            v4u d[4];
#pragma unroll
            for (int i = 0; i < 4; i++) {
                const int ch = (i & 1) * 32 + (tid >> 3), ck = tid & 7;
                d[i] = *(LAS v4u*)(sm + TILE_OFF + (i >> 1) * 8192 + ch * 128 + ((ck ^ ((ch >> 1) & 7)) * 16));
            }
#pragma unroll
            for (int i = 0; i < 4; i++) {
                const int ch = (i & 1) * 32 + (tid >> 3), ck = tid & 7;
                __builtin_amdgcn_raw_buffer_store_b128(d[i], ut_rsrc, (u32)((((i >> 1) * D_ + dg * 64 + ch) * S_ + tg * 64 + ck * 8) * 2), 0, 16);
            }
        }
        STAMP(3);
        const u64 epoch = (u64)__builtin_amdgcn_readfirstlane((u32)E) + ((u64)__builtin_amdgcn_readfirstlane((u32)(E >> 32)) << 32) + 1ull;
        u32 gb;
        {
            float s0 = 0.f, s1 = 0.f;
#pragma unroll
            for (int j = 0; j < 4; j++) {
                u32 p0 = pk(bflo(gx0[j]) * bflo(gw[j]), bfhi(gx0[j]) * bfhi(gw[j]));
                u32 p1 = pk(bflo(gx1[j]) * bflo(gw[j]), bfhi(gx1[j]) * bfhi(gw[j]));
                s0 += bflo(p0) + bfhi(p0);
                s1 += bflo(p1) + bfhi(p1);
            }
            s0 = dpp_add<0x111>(s0); s1 = dpp_add<0x111>(s1);
            s0 = dpp_add<0x112>(s0); s1 = dpp_add<0x112>(s1);
            s0 = dpp_add<0x114>(s0); s1 = dpp_add<0x114>(s1);
            s0 = dpp_add<0x118>(s0); s1 = dpp_add<0x118>(s1);
            const int i0 = __float_as_int(s0), i1 = __float_as_int(s1);
            const float t0_ = __int_as_float(__builtin_amdgcn_readlane(i0, 15)) + __int_as_float(__builtin_amdgcn_readlane(i0, 31)) +
                              __int_as_float(__builtin_amdgcn_readlane(i0, 47)) + __int_as_float(__builtin_amdgcn_readlane(i0, 63));
            const float t1_ = __int_as_float(__builtin_amdgcn_readlane(i1, 15)) + __int_as_float(__builtin_amdgcn_readlane(i1, 31)) +
                              __int_as_float(__builtin_amdgcn_readlane(i1, 47)) + __int_as_float(__builtin_amdgcn_readlane(i1, 63));
            const float z = rbf((lane & 1) ? t1_ : t0_);
            const float g = 1.f / (1.f + __expf(-z));
            gb = pk(g, g) & 0xffff;
        }
        asm volatile("s_waitcnt vmcnt(0)" ::: "memory");
        STAMP(4);
        if (lane < 8) {
            const int cons = dg * 32 + (lane >> 2) * 16 + w * 4 + (lane & 3);
            __hip_atomic_store((GAS u64*)(ws + WS_INBOX) + cons * 32 + tg, epoch, __ATOMIC_RELAXED, __HIP_MEMORY_SCOPE_AGENT);
        }
        if (lane < 2) __hip_atomic_store((GAS u64*)(ws + WS_GATE) + tgate + lane, (epoch << 16) | gb, __ATOMIC_RELAXED, __HIP_MEMORY_SCOPE_AGENT);
        STAMP(5);
        {
            const int pidx = w * 8 - 1 + min(lane, 8);
            GAS u64* inbox = (GAS u64*)(ws + WS_INBOX) + p * 32 + max(pidx, 0);
            u64 va = __hip_atomic_load(inbox, __ATOMIC_RELAXED, __HIP_MEMORY_SCOPE_AGENT);
#pragma unroll 1
            for (u32 spins = 0; spins < SPIN_CAP; spins++) {
                const u64 vb = __hip_atomic_load(inbox, __ATOMIC_RELAXED, __HIP_MEMORY_SCOPE_AGENT);
                if (__all(va == epoch)) break;
                va = __hip_atomic_load(inbox, __ATOMIC_RELAXED, __HIP_MEMORY_SCOPE_AGENT);
                if (__all(vb == epoch)) break;
            }
            asm volatile("" ::: "memory");
            STAMP(6);
            v4u ud[4];
            u32 up[4];
            const int t = w * 512 + lane * 8;
#pragma unroll
            for (int u = 0; u < 4; u++) {
                const u32 off = (u32)((((u & 1) * D_ + p * 2 + (u >> 1)) * S_ + t) * 2);
                ud[u] = __builtin_amdgcn_raw_buffer_load_b128(ut_rsrc, off, 0, 16);
                up[u] = __builtin_amdgcn_raw_buffer_load_b32(ut_rsrc, t ? off - 4u : off, 0, 16);
            }
            wait_lds(cnt + 24, 8);
            *(LAS v4u*)(sm + U_OFF + (tid >> 5) * U_COPY + (tid & 31) * 16) = (v4u){0, 0, 0, 0};
#pragma unroll
            for (int u = 0; u < 4; u++) {
                const lptr dst = sm + U_OFF + u * U_UNIT + 512 + t * 2;
                const u32 prev = t ? up[u] : 0u;
                *(LAS v4u*)(dst + U_COPY) = ud[u];
                *(LAS v4u*)(dst) = (v4u){__builtin_amdgcn_alignbit(ud[u][0], prev, 16), __builtin_amdgcn_alignbit(ud[u][1], ud[u][0], 16),
                                         __builtin_amdgcn_alignbit(ud[u][2], ud[u][1], 16), __builtin_amdgcn_alignbit(ud[u][3], ud[u][2], 16)};
            }
        }
        STAMP(7);
        RAW_BARRIER();
        STAMP(8);
        __builtin_amdgcn_s_setprio(0);
        const int g4 = lane >> 4, n = lane & 15;
        const lptr ha = sm + w * H_UNIT + (n & 3) * H_COPY + 48 - 16 * (n >> 2) + 16 * g4;
        const lptr ub = sm + U_OFF + w * U_UNIT + (n & 1) * U_COPY + 512 + 64 * (n >> 1) + 16 * g4 - 7 * 64;
        const lptr yb = sm + Y_OFF + w * 4096 + 64 * (n >> 1) + 16 * g4 + 8 * (n & 1);
        v8bf A0[8], B0[8], A1[8], B1[8];
        v4f acc[8];
#pragma unroll
        for (int i = 0; i < 8; i++) acc[i] = (v4f){0.f, 0.f, 0.f, 0.f};
        load_set<0>(A0, B0, ha, ub);
        conv_steps<0>(A0, B0, A1, B1, acc, ha, ub, yb, cnt
        );
        STAMP_FLUSH();
    } else {
        const int kt = tid - 256;
        const int half = __builtin_amdgcn_readfirstlane(kt >> 8);
        const int kch = (kt >> 7) & 1, kn = kt & 127;
        const int kd = p * 2 + kch;
        const u32 pi = (u32)(kd * N_ + kn) * 2u;
        const u16 p_ldt = ldg<u16>(logdt, (u32)kd * 2u);
        const u16 p_are = ldg<u16>(Are, pi), p_aim = ldg<u16>(Aim, pi), p_bre = ldg<u16>(Bre, pi), p_bim = ldg<u16>(Bim, pi), p_cre = ldg<u16>(Cre, pi), p_cim = ldg<u16>(Cim, pi);
        const int hck = kt & 255;
        const u32 ho = (u32)((p * 2 + half) * S_ + hck * 8) * 2u;
        v4u hd;
        v3u hm;
        u64 E;
        const u32 dAp = *(const u32*)(ssmD + p * 2), dSp = *(const u32*)(Dskip + p * 2);
        __builtin_amdgcn_sched_barrier(0);
        STAMP(1);
        u32 lv[14];
#pragma unroll
        for (int j = 0; j < 14; j++) {
            const int l = hck * 8 - 6 + j;
            lv[j] = (u32)rbf((float)(j < 6 ? max(l, 0) : l)) * 2;
        }
        {
            const float dt = rbf(expf(bf1(p_ldt)));
            const float a = dt * bf1(p_are), b = dt * bf1(p_aim);
            const float br = dt * bf1(p_bre), bi = dt * bf1(p_bim);
            const float cre = bf1(p_cre), cim = bf1(p_cim);
            v2f CB;
            CB[0] = cre * br - cim * bi;
            CB[1] = -(cre * bi + cim * br);
            const float a2 = a * 1.4426950408889634f;
            const float rh = b * 0.15915494309189535f;
            const float rl = __builtin_fmaf(b, 0.15915494309189535f, -rh) + b * 6.4206383e-9f;
            const lptr tab = sm + R2_OFF + kch * TAB_CH + kn * 4;
            asm volatile("" :: "v"(a2), "v"(rh), "v"(rl), "v"(CB), "v"(pf));
            __builtin_amdgcn_sched_barrier(0);
            STAMP(2);
            hd = ldg<v4u>(hA, ho);
            hm = ldg<v3u>(hA, ho - (hck ? 12u : 0u));
            E = __hip_atomic_load((GAS u64*)(ws + WS_E), __ATOMIC_RELAXED, __HIP_MEMORY_SCOPE_AGENT);
            __builtin_amdgcn_sched_barrier(0);
#define PUT(row, v) (*(LAS u32*)(tab + (row) * TAB_PITCH) = pk2(v))
#define PUTC(row, v) (*(LAS u32*)(tab + (row) * TAB_PITCH) = pk((v)[0], -(v)[1]))
            v2f t0_, t1_;
            if (half == 0) {
                v2f E0 = epow<true>(a2, rh, rl, 1.f), E3 = epow<true>(a2, rh, rl, 8.f), E7 = epow<true>(a2, rh, rl, 128.f);
                v2f E1, E2, F[8], F8;
                csqr2(E1, E0, F[2], E3);
                csqr2(E2, E1, F[4], F[2]);
                v2f P[8];
                P[0] = CB; F[1] = E3;
                cmul2(P[1], P[0], E0, P[2], P[0], E1);
                cmul2(P[3], P[1], E1, P[4], P[0], E2);
                cmul2(P[5], P[1], E2, P[6], P[2], E2);
                cmul2(P[7], P[3], E2, F8, F[4], F[4]);
#pragma unroll
                for (int e = 0; e < 8; e += 2) {
                    PUT(e, P[e]); PUT(e + 1, P[e + 1]);
                    cmul2(t0_, P[e], E7, t1_, P[e + 1], E7);
                    PUT(8 + e, t0_); PUT(9 + e, t1_);
                }
                F[0][0] = 1.f; F[0][1] = 0.f;
                cmul2(F[3], F[1], F[2], F[5], F[4], F[1]);
                cmul2(F[6], F[4], F[2], t0_, F8, F[1]);
                PUTC(48 + 9, t0_);
                cmul2(F[7], F[4], F[3], t0_, F8, F[2]);
                PUTC(48 + 10, t0_);
#pragma unroll
                for (int e = 0; e < 8; e++) PUTC(48 + e, F[e]);
                PUTC(48 + 8, F8);
                cmul2(t0_, F8, F[3], t1_, F8, F[4]); PUTC(48 + 11, t0_); PUTC(48 + 12, t1_);
                cmul2(t0_, F8, F[5], t1_, F8, F[6]); PUTC(48 + 13, t0_); PUTC(48 + 14, t1_);
                cmul2(t0_, F8, F[7], t1_, F8, F[7]); PUTC(48 + 15, t0_);
            } else {
                v2f E1 = epow<true>(a2, rh, rl, 2.f), E7 = epow<true>(a2, rh, rl, 128.f), E9 = epow<true>(a2, rh, rl, 512.f);
                v2f E2, E8, E10, E11, P2, P4, P6, W3, R[4], Q0, Q1, T0, T1, T2, T3;
                csqr2(E2, E1, E8, E7);
                cmul2(E10, E9, E9, P2, CB, E1);
                cmul2(E11, E10, E10, P4, CB, E2);
                cmul2(P6, P2, E2, W3, E7, E8);
                cmul2(R[0], CB, E8, R[1], P2, E8);
                cmul2(R[2], P4, E8, R[3], P6, E8);
#pragma unroll
                for (int e = 0; e < 4; e += 2) {
                    PUT(16 + e, R[e]); PUT(17 + e, R[e + 1]);
                    cmul2(t0_, R[e], E7, t1_, R[e + 1], E7);
                    PUT(20 + e, t0_); PUT(21 + e, t1_);
                }
                cmul2(Q0, CB, E9, Q1, P4, E9);
                PUT(24, Q0); PUT(28, Q1);
                cmul2(t0_, Q0, E7, t1_, Q1, E7); PUT(25, t0_); PUT(29, t1_);
                cmul2(t0_, Q0, E8, t1_, Q1, E8); PUT(26, t0_); PUT(30, t1_);
                cmul2(t0_, Q0, W3, t1_, Q1, W3); PUT(27, t0_); PUT(31, t1_);
                cmul2(T0, CB, E10, t0_, CB, E11); PUT(32, T0); PUT(40, t0_);
                cmul2(T1, T0, E7, T2, T0, E8); PUT(33, T1); PUT(34, T2);
                cmul2(T3, T0, W3, t0_, T0, E9); PUT(35, T3); PUT(36, t0_);
                cmul2(t0_, T1, E9, t1_, T2, E9); PUT(37, t0_); PUT(38, t1_);
                cmul2(t0_, T3, E9, t1_, T3, E9); PUT(39, t0_);
            }
        }
        lds_arrive(cnt + 20);
        STAMP(3);
        {
            if (hck == 0) { hm[0] = 0; hm[1] = 0; hm[2] = 0; }
            u32 r[7];
            r[0] = rot16(hd[3]); r[1] = rot16(hd[2]); r[2] = rot16(hd[1]); r[3] = rot16(hd[0]);
            r[4] = rot16(hm[2]); r[5] = rot16(hm[1]); r[6] = rot16(hm[0]);
            const lptr dst = sm + (half * 2) * H_UNIT + (255 - hck) * 16;
            *(LAS v4u*)(dst) = (v4u){r[3], r[4], r[5], r[6]};
            *(LAS v4u*)(dst + H_COPY) = (v4u){r[2], r[3], r[4], r[5]};
            *(LAS v4u*)(dst + 2 * H_COPY) = (v4u){r[1], r[2], r[3], r[4]};
            *(LAS v4u*)(dst + 3 * H_COPY) = (v4u){r[0], r[1], r[2], r[3]};
        }
        if (kt < 48) {
            const int uc = kt / 3, ck = kt - uc * 3;
            *(LAS v4u*)(sm + uc * H_COPY + (256 + ck) * 16) = (v4u){0, 0, 0, 0};
        }
        const int id = w - 4;
        if (id < 6) {
            const int c2 = (id >= 3) ? 1 : 0, mt = id - c2 * 3;
            const int g4 = lane >> 4;
            int base, step;
            if (mt == 0) { base = ((g4 >> 1) << 7) + ((g4 & 1) << 2); step = 1; }
            else if (mt == 1) { base = (g4 < 2) ? (256 + (g4 << 7)) : (512 + ((g4 & 1) << 2)); step = (g4 < 2) ? 2 : 128; }
            else { base = (g4 < 2) ? (1024 + (g4 << 9)) : (2048 + ((g4 & 1) << 3)); step = 128; }
            const lptr T = sm + T_OFF + c2 * T_CH + (base + 8 * (lane & 15)) * 2;
            const lptr ga = sm + R2_OFF + c2 * TAB_CH + (mt * 16 + (lane & 15)) * TAB_PITCH + (lane >> 4) * 16;
            const lptr fb = sm + R2_OFF + c2 * TAB_CH + (48 + (lane & 15)) * TAB_PITCH + (lane >> 4) * 16;
            wait_lds(cnt + 20, 8);
            STAMP(4);
            v8bf A[8], B[8];
#pragma unroll
            for (int s = 0; s < 8; s++) { A[s] = *(LAS v8bf*)(ga + s * 64); B[s] = *(LAS v8bf*)(fb + s * 64); }
            v4f acc0 = {0.f, 0.f, 0.f, 0.f}, acc1 = {0.f, 0.f, 0.f, 0.f};
#pragma unroll
            for (int s = 0; s < 8; s += 2) {
                acc0 = __builtin_amdgcn_mfma_f32_16x16x32_bf16(A[s], B[s], acc0, 0, 0, 0);
                acc1 = __builtin_amdgcn_mfma_f32_16x16x32_bf16(A[s + 1], B[s + 1], acc1, 0, 0, 0);
            }
            const u32 o01 = pk(acc0[0] + acc1[0], acc0[1] + acc1[1]), o23 = pk(acc0[2] + acc1[2], acc0[3] + acc1[3]);
            *(LAS u16*)(T) = (u16)o01;
            *(LAS u16*)(T + step * 2) = (u16)(o01 >> 16);
            *(LAS u16*)(T + step * 4) = (u16)o23;
            *(LAS u16*)(T + step * 6) = (u16)(o23 >> 16);
        }
        lds_arrive(cnt + 24);
        wait_lds(cnt + 24, 8);
        STAMP(5);
        {
            const lptr T = sm + T_OFF + half * T_CH;
            u32 hv[14];
#pragma unroll
            for (int j = 0; j < 14; j++) {
                const u32 x = *(LAS u16*)(T + lv[j]);
                hv[j] = (j < 6 && hck == 0) ? 0u : x;
            }
            u32 r[7];
#pragma unroll
            for (int i = 0; i < 7; i++) r[i] = hv[13 - 2 * i] | (hv[12 - 2 * i] << 16);
            const lptr dst = sm + (half * 2 + 1) * H_UNIT + (255 - hck) * 16;
            *(LAS v4u*)(dst) = (v4u){r[3], r[4], r[5], r[6]};
            *(LAS v4u*)(dst + H_COPY) = (v4u){r[2], r[3], r[4], r[5]};
            *(LAS v4u*)(dst + 2 * H_COPY) = (v4u){r[1], r[2], r[3], r[4]};
            *(LAS v4u*)(dst + 3 * H_COPY) = (v4u){r[0], r[1], r[2], r[3]};
        }
        STAMP(6);
        const u64 epoch = (u64)__builtin_amdgcn_readfirstlane((u32)E) + ((u64)__builtin_amdgcn_readfirstlane((u32)(E >> 32)) << 32) + 1ull;
        const float dA0 = bflo(dAp), dA1 = bfhi(dAp), dS0 = bflo(dSp), dS1 = bfhi(dSp);
        RAW_BARRIER();
        __builtin_amdgcn_s_setprio(3);
        STAMP(8);
        {
            GAS u64* gr = (GAS u64*)(ws + WS_GATE) + kt;
            const u64 tag = epoch & 0xFFFFFFFFFFFFull;
            u32 gv[4];
#pragma unroll 1
            for (u32 spins = 0; spins < SPIN_CAP; spins++) {
                bool ok = true;
#pragma unroll
                for (int i = 0; i < 4; i++) {
                    const u64 v = __hip_atomic_load(gr + i * 512, __ATOMIC_RELAXED, __HIP_MEMORY_SCOPE_AGENT);
                    gv[i] = (u32)v & 0xffff;
                    ok &= (v >> 16) == tag;
                }
                if (__all(ok)) break;
                __builtin_amdgcn_s_sleep(1);
            }
#pragma unroll
            for (int i = 0; i < 4; i++) {
                const int t = kt + i * 512;
                *(LAS u32*)(sm + G_OFF + (t >> 3) * 32 + (t & 1) * 16 + ((t >> 1) & 3) * 4) = gv[i] | (pk(0.f, 1.f - bf1((u16)gv[i])) & 0xffff0000u);
            }
        }
        lds_arrive(cnt + 28);
        wait_lds(cnt + 28, 8);
        if (bid == 0 && tid == 256) __hip_atomic_store((GAS u64*)(ws + WS_E), epoch, __ATOMIC_RELAXED, __HIP_MEMORY_SCOPE_AGENT);
        STAMP(7);
        wait_lds(cnt, 4);
        STAMP(9);
        if (kt < PASS_SPLIT * 64) epi_quad(sm, (kt >> 1) * 8, kt & 1, out32, p, dA0, dA1, dS0, dS1);
        STAMP(11);
        const int tb = 511 - kt;
        if (w >= 12 - ((8 - PASS_SPLIT) * 64 + 63) / 64) {
            wait_lds(cnt + 4, 4);
            STAMP(10);
            if (tb < (8 - PASS_SPLIT) * 64) epi_quad(sm, PASS_SPLIT * 256 + (tb >> 1) * 8, tb & 1, out32, p, dA0, dA1, dS0, dS1);
            STAMP(13);
        }
        STAMP(12);
        STAMP_FLUSH();
    }
}

extern "C" void kernel_launch(void* const* d_in, const int* in_sizes, int n_in,
                              void* d_out, int out_size, void* d_ws, size_t ws_size,
                              hipStream_t stream) {
    fused_kernel<<<256, NTHREADS, 0, stream>>>((const u16*)d_in[0], (const u16*)d_in[1], (const u16*)d_in[2], (const u16*)d_in[3],
                                          (const u16*)d_in[4], (const u16*)d_in[5], (const u16*)d_in[7], (const u16*)d_in[8],
                                          (const u16*)d_in[9], (const u16*)d_in[10], (const u16*)d_in[11], (const u16*)d_in[12],
                                          (const u16*)d_in[13], (const u16*)d_in[14], (const u16*)d_in[15], (char*)d_ws, (u32*)d_out);
}
```

```cpp
#include <hip/hip_runtime.h>
#include <stdint.h>

typedef unsigned short u16;
typedef unsigned int u32;
typedef unsigned long long u64;
typedef __bf16 v8bf __attribute__((ext_vector_type(8)));
typedef __bf16 v2bf __attribute__((ext_vector_type(2)));
typedef float v4f __attribute__((ext_vector_type(4)));
typedef float v2f __attribute__((ext_vector_type(2)));
typedef u32 v4u __attribute__((ext_vector_type(4)));
typedef u32 v2u __attribute__((ext_vector_type(2)));
typedef u32 v3u __attribute__((ext_vector_type(3)));
#define LAS __attribute__((address_space(3)))
#define GAS __attribute__((address_space(1)))
typedef LAS char* lptr;

#define S_ 2048
#define D_ 512
#define N_ 128

__device__ __forceinline__ float bflo(u32 v) { return __uint_as_float(v << 16); }
__device__ __forceinline__ float bfhi(u32 v) { return __uint_as_float(v & 0xffff0000u); }
__device__ __forceinline__ float bf1(u16 v) { return __uint_as_float(((u32)v) << 16); }
__device__ __forceinline__ u32 pk(float lo, float hi) {
    v2f x = {lo, hi};
    return __builtin_bit_cast(u32, __builtin_convertvector(x, v2bf));
}
__device__ __forceinline__ u32 pk2(v2f x) { return __builtin_bit_cast(u32, __builtin_convertvector(x, v2bf)); }
__device__ __forceinline__ u32 rot16(u32 v) { return __builtin_amdgcn_alignbit(v, v, 16); }
__device__ __forceinline__ u64 uniform64(u64 v) {
    const u32 lo = (u32)__builtin_amdgcn_readfirstlane((u32)v), hi = (u32)__builtin_amdgcn_readfirstlane((u32)(v >> 32));
    return (u64)lo | ((u64)hi << 32);
}
__device__ __forceinline__ float rbf(float f) { return bflo(pk(f, f)); }

template <typename T>
__device__ __forceinline__ T ldg(const void* base, u32 off) { return *(const GAS T*)((const GAS char*)base + off); }

__device__ __forceinline__ void cmul2(v2f& c0, v2f a0, v2f b0, v2f& c1, v2f a1, v2f b1) {
    asm("v_pk_mul_f32 %0, %2, %3 op_sel:[0,0] op_sel_hi:[1,0]\n\t"
        "v_pk_mul_f32 %1, %4, %5 op_sel:[0,0] op_sel_hi:[1,0]\n\t"
        "v_pk_fma_f32 %0, %2, %3, %0 op_sel:[1,1,0] op_sel_hi:[0,1,1] neg_lo:[1,0,0]\n\t"
        "v_pk_fma_f32 %1, %4, %5, %1 op_sel:[1,1,0] op_sel_hi:[0,1,1] neg_lo:[1,0,0]"
        : "=&v"(c0), "=&v"(c1) : "v"(a0), "v"(b0), "v"(a1), "v"(b1));
}

__device__ __forceinline__ void csqr2(v2f& c0, v2f a0, v2f& c1, v2f a1) { cmul2(c0, a0, a0, c1, a1, a1); }

template <bool CONJ>
__device__ __forceinline__ v2f epow(float a2, float rh, float rl, float s) {
    float e = __builtin_amdgcn_exp2f(a2 * s);
    float x = __builtin_fmaf(rl, s, __builtin_amdgcn_fractf(rh * s));
    float cs = __builtin_amdgcn_cosf(x), sn = __builtin_amdgcn_sinf(x);
    v2f c;
    c[0] = e * cs;
    c[1] = CONJ ? -(e * sn) : (e * sn);
    return c;
}

#define H_COPY 4160
#define H_UNIT (4 * H_COPY)
#define R2_OFF (4 * H_UNIT)
#define TAB_PITCH 544
#define TAB_CH (64 * TAB_PITCH)
#define U_OFF R2_OFF
#define U_COPY 4640
#define U_UNIT (2 * U_COPY)
#define Y_OFF (U_OFF + 4 * U_UNIT)
#define T_OFF (R2_OFF + 2 * TAB_CH)
#define T_CH 5248
#define TILE_OFF (T_OFF + 2 * T_CH)
#define F_OFF (TILE_OFF + 16384)
#define EP_OFF (F_OFF + 64)
#define LDS_BYTES (F_OFF + 80)
#define C_EP 28
#define C_TILE 32
#define C_TAB 36
#define C_T 40
#define C_G 44
#define C_H 48
#define C_UB 52
#define C_UA 56
#define C_TR 60

#define WS_E 0
#define WS_INBOX 4096
#define WS_GATE (128 * 1024)
#define WS_UT (1024 * 1024)

#define SPIN_CAP (1u << 20)
#define RAW_BARRIER() do { asm volatile("s_waitcnt lgkmcnt(0)" ::: "memory"); __builtin_amdgcn_s_barrier(); asm volatile("" ::: "memory"); } while (0)

template <int CTRL>
__device__ __forceinline__ float dpp_add(float v) {
    int t = __builtin_amdgcn_update_dpp(0, __float_as_int(v), CTRL, 0xf, 0xf, true);
    return v + __int_as_float(t);
}

__device__ __forceinline__ void lds_arrive(lptr word) {
    asm volatile("s_waitcnt lgkmcnt(0)" ::: "memory");
    if ((threadIdx.x & 63) == 0) __hip_atomic_fetch_add((LAS u32*)word, 1u, __ATOMIC_RELAXED, __HIP_MEMORY_SCOPE_WORKGROUP);
}
__device__ __forceinline__ void wait_lds(lptr word, u32 want) {
#pragma unroll 1
    for (u32 spins = 0; spins < SPIN_CAP; spins++) {
        const u32 v = __hip_atomic_load((LAS u32*)word, __ATOMIC_RELAXED, __HIP_MEMORY_SCOPE_WORKGROUP);
        if (__builtin_amdgcn_readfirstlane(v) >= want) break;
        __builtin_amdgcn_s_sleep(1);
    }
    asm volatile("" ::: "memory");
}

#define CONV_ASM \
    "s_waitcnt lgkmcnt(0)\n\t" \
    "ds_read_b128 v[80:83], %1 offset:4032\n\t" \
    "ds_read_b128 v[88:91], %2 offset:960\n\t" \
    "ds_read_b128 v[92:95], %1 offset:3520\n\t" \
    "ds_read_b128 v[84:87], %2 offset:1472\n\t" \
    "ds_read_b128 v[112:115], %2 offset:448\n\t" \
    "ds_read_b128 v[116:119], %1 offset:3008\n\t" \
    "ds_read_b128 v[120:123], %1 offset:2496\n\t" \
    "ds_read_b128 v[124:127], %2 offset:1984\n\t" \
    "s_waitcnt lgkmcnt(6)\n\t" \
    "v_mfma_f32_16x16x32_bf16 v[132:135], v[80:83], v[88:91], 0\n\t" \
    "ds_read_b128 v[64:67], %1 offset:3968\n\t" \
    "s_waitcnt lgkmcnt(5)\n\t" \
    "v_mfma_f32_16x16x32_bf16 v[140:143], v[92:95], v[84:87], 0\n\t" \
    "ds_read_b128 v[100:103], %2 offset:896\n\t" \
    "v_mfma_f32_16x16x32_bf16 v[136:139], v[92:95], v[88:91], 0\n\t" \
    "ds_read_b128 v[68:71], %1 offset:3456\n\t" \
    "s_waitcnt lgkmcnt(6)\n\t" \
    "v_mfma_f32_16x16x32_bf16 v[128:131], v[80:83], v[112:115], 0\n\t" \
    "ds_read_b128 v[104:107], %2 offset:1408\n\t" \
    "s_waitcnt lgkmcnt(6)\n\t" \
    "v_mfma_f32_16x16x32_bf16 v[140:143], v[116:119], v[88:91], v[140:143]\n\t" \
    "ds_read_b128 v[96:99], %2 offset:384\n\t" \
    "v_mfma_f32_16x16x32_bf16 v[132:135], v[92:95], v[112:115], v[132:135]\n\t" \
    "ds_read_b128 v[72:75], %1 offset:2944\n\t" \
    "v_mfma_f32_16x16x32_bf16 v[136:139], v[116:119], v[112:115], v[136:139]\n\t" \
    "ds_read_b128 v[76:79], %1 offset:2432\n\t" \
    "s_waitcnt lgkmcnt(8)\n\t" \
    "v_mfma_f32_16x16x32_bf16 v[140:143], v[120:123], v[112:115], v[140:143]\n\t" \
    "ds_read_b128 v[108:111], %2 offset:1920\n\t" \
    "s_waitcnt lgkmcnt(8)\n\t" \
    "v_mfma_f32_16x16x32_bf16 v[140:143], v[80:83], v[124:127], v[140:143]\n\t" \
    "v_mfma_f32_16x16x32_bf16 v[136:139], v[80:83], v[84:87], v[136:139]\n\t" \
    "s_waitcnt lgkmcnt(6)\n\t" \
    "v_mfma_f32_16x16x32_bf16 v[132:135], v[64:67], v[100:103], v[132:135]\n\t" \
    "ds_read_b128 v[80:83], %1 offset:3904\n\t" \
    "s_waitcnt lgkmcnt(5)\n\t" \
    "v_mfma_f32_16x16x32_bf16 v[140:143], v[68:71], v[104:107], v[140:143]\n\t" \
    "ds_read_b128 v[88:91], %2 offset:832\n\t" \
    "v_mfma_f32_16x16x32_bf16 v[136:139], v[68:71], v[100:103], v[136:139]\n\t" \
    "ds_read_b128 v[92:95], %1 offset:3392\n\t" \
    "s_waitcnt lgkmcnt(6)\n\t" \
    "v_mfma_f32_16x16x32_bf16 v[128:131], v[64:67], v[96:99], v[128:131]\n\t" \
    "ds_read_b128 v[84:87], %2 offset:1344\n\t" \
    "s_waitcnt lgkmcnt(6)\n\t" \
    "v_mfma_f32_16x16x32_bf16 v[140:143], v[72:75], v[100:103], v[140:143]\n\t" \
    "ds_read_b128 v[112:115], %2 offset:320\n\t" \
    "v_mfma_f32_16x16x32_bf16 v[132:135], v[68:71], v[96:99], v[132:135]\n\t" \
    "ds_read_b128 v[116:119], %1 offset:2880\n\t" \
    "v_mfma_f32_16x16x32_bf16 v[136:139], v[72:75], v[96:99], v[136:139]\n\t" \
    "ds_read_b128 v[120:123], %1 offset:2368\n\t" \
    "s_waitcnt lgkmcnt(8)\n\t" \
    "v_mfma_f32_16x16x32_bf16 v[140:143], v[76:79], v[96:99], v[140:143]\n\t" \
    "ds_read_b128 v[124:127], %2 offset:1856\n\t" \
    "s_waitcnt lgkmcnt(8)\n\t" \
    "v_mfma_f32_16x16x32_bf16 v[140:143], v[64:67], v[108:111], v[140:143]\n\t" \
    "v_mfma_f32_16x16x32_bf16 v[136:139], v[64:67], v[104:107], v[136:139]\n\t" \
    "s_waitcnt lgkmcnt(6)\n\t" \
    "v_mfma_f32_16x16x32_bf16 v[132:135], v[80:83], v[88:91], v[132:135]\n\t" \
    "ds_read_b128 v[64:67], %1 offset:3840\n\t" \
    "s_waitcnt lgkmcnt(5)\n\t" \
    "v_mfma_f32_16x16x32_bf16 v[140:143], v[92:95], v[84:87], v[140:143]\n\t" \
    "ds_read_b128 v[100:103], %2 offset:768\n\t" \
    "v_mfma_f32_16x16x32_bf16 v[136:139], v[92:95], v[88:91], v[136:139]\n\t" \
    "ds_read_b128 v[68:71], %1 offset:3328\n\t" \
    "s_waitcnt lgkmcnt(6)\n\t" \
    "v_mfma_f32_16x16x32_bf16 v[128:131], v[80:83], v[112:115], v[128:131]\n\t" \
    "ds_read_b128 v[104:107], %2 offset:1280\n\t" \
    "s_waitcnt lgkmcnt(6)\n\t" \
    "v_mfma_f32_16x16x32_bf16 v[140:143], v[116:119], v[88:91], v[140:143]\n\t" \
    "ds_read_b128 v[96:99], %2 offset:256\n\t" \
    "v_mfma_f32_16x16x32_bf16 v[132:135], v[92:95], v[112:115], v[132:135]\n\t" \
    "ds_read_b128 v[72:75], %1 offset:2816\n\t" \
    "v_mfma_f32_16x16x32_bf16 v[136:139], v[116:119], v[112:115], v[136:139]\n\t" \
    "ds_read_b128 v[76:79], %1 offset:2304\n\t" \
    "s_waitcnt lgkmcnt(8)\n\t" \
    "v_mfma_f32_16x16x32_bf16 v[140:143], v[120:123], v[112:115], v[140:143]\n\t" \
    "ds_read_b128 v[108:111], %2 offset:1792\n\t" \
    "s_waitcnt lgkmcnt(8)\n\t" \
    "v_mfma_f32_16x16x32_bf16 v[140:143], v[80:83], v[124:127], v[140:143]\n\t" \
    "v_mfma_f32_16x16x32_bf16 v[136:139], v[80:83], v[84:87], v[136:139]\n\t" \
    "s_waitcnt lgkmcnt(6)\n\t" \
    "v_mfma_f32_16x16x32_bf16 v[132:135], v[64:67], v[100:103], v[132:135]\n\t" \
    "ds_read_b128 v[80:83], %1 offset:3776\n\t" \
    "s_waitcnt lgkmcnt(5)\n\t" \
    "v_mfma_f32_16x16x32_bf16 v[140:143], v[68:71], v[104:107], v[140:143]\n\t" \
    "ds_read_b128 v[88:91], %2 offset:704\n\t" \
    "v_mfma_f32_16x16x32_bf16 v[136:139], v[68:71], v[100:103], v[136:139]\n\t" \
    "ds_read_b128 v[92:95], %1 offset:3264\n\t" \
    "s_waitcnt lgkmcnt(6)\n\t" \
    "v_mfma_f32_16x16x32_bf16 v[128:131], v[64:67], v[96:99], v[128:131]\n\t" \
    "ds_read_b128 v[84:87], %2 offset:1216\n\t" \
    "s_waitcnt lgkmcnt(6)\n\t" \
    "v_mfma_f32_16x16x32_bf16 v[140:143], v[72:75], v[100:103], v[140:143]\n\t" \
    "ds_read_b128 v[112:115], %2 offset:192\n\t" \
    "v_mfma_f32_16x16x32_bf16 v[132:135], v[68:71], v[96:99], v[132:135]\n\t" \
    "ds_read_b128 v[116:119], %1 offset:2752\n\t" \
    "v_mfma_f32_16x16x32_bf16 v[136:139], v[72:75], v[96:99], v[136:139]\n\t" \
    "ds_read_b128 v[120:123], %1 offset:2240\n\t" \
    "s_waitcnt lgkmcnt(8)\n\t" \
    "v_mfma_f32_16x16x32_bf16 v[140:143], v[76:79], v[96:99], v[140:143]\n\t" \
    "ds_read_b128 v[124:127], %2 offset:1728\n\t" \
    "s_waitcnt lgkmcnt(8)\n\t" \
    "v_mfma_f32_16x16x32_bf16 v[140:143], v[64:67], v[108:111], v[140:143]\n\t" \
    "v_mfma_f32_16x16x32_bf16 v[136:139], v[64:67], v[104:107], v[136:139]\n\t" \
    "s_waitcnt lgkmcnt(6)\n\t" \
    "v_mfma_f32_16x16x32_bf16 v[132:135], v[80:83], v[88:91], v[132:135]\n\t" \
    "ds_read_b128 v[64:67], %1 offset:3712\n\t" \
    "s_waitcnt lgkmcnt(5)\n\t" \
    "v_mfma_f32_16x16x32_bf16 v[140:143], v[92:95], v[84:87], v[140:143]\n\t" \
    "ds_read_b128 v[100:103], %2 offset:640\n\t" \
    "v_mfma_f32_16x16x32_bf16 v[136:139], v[92:95], v[88:91], v[136:139]\n\t" \
    "ds_read_b128 v[68:71], %1 offset:3200\n\t" \
    "s_waitcnt lgkmcnt(6)\n\t" \
    "v_mfma_f32_16x16x32_bf16 v[128:131], v[80:83], v[112:115], v[128:131]\n\t" \
    "ds_read_b128 v[104:107], %2 offset:1152\n\t" \
    "s_waitcnt lgkmcnt(6)\n\t" \
    "v_mfma_f32_16x16x32_bf16 v[140:143], v[116:119], v[88:91], v[140:143]\n\t" \
    "ds_read_b128 v[96:99], %2 offset:128\n\t" \
    "v_mfma_f32_16x16x32_bf16 v[132:135], v[92:95], v[112:115], v[132:135]\n\t" \
    "ds_read_b128 v[72:75], %1 offset:2688\n\t" \
    "v_mfma_f32_16x16x32_bf16 v[136:139], v[116:119], v[112:115], v[136:139]\n\t" \
    "ds_read_b128 v[76:79], %1 offset:2176\n\t" \
    "s_waitcnt lgkmcnt(8)\n\t" \
    "v_mfma_f32_16x16x32_bf16 v[140:143], v[120:123], v[112:115], v[140:143]\n\t" \
    "ds_read_b128 v[108:111], %2 offset:1664\n\t" \
    "s_waitcnt lgkmcnt(8)\n\t" \
    "v_mfma_f32_16x16x32_bf16 v[140:143], v[80:83], v[124:127], v[140:143]\n\t" \
    "v_mfma_f32_16x16x32_bf16 v[136:139], v[80:83], v[84:87], v[136:139]\n\t" \
    "s_waitcnt lgkmcnt(6)\n\t" \
    "v_mfma_f32_16x16x32_bf16 v[132:135], v[64:67], v[100:103], v[132:135]\n\t" \
    "ds_read_b128 v[80:83], %1 offset:3648\n\t" \
    "s_waitcnt lgkmcnt(5)\n\t" \
    "v_mfma_f32_16x16x32_bf16 v[140:143], v[68:71], v[104:107], v[140:143]\n\t" \
    "ds_read_b128 v[88:91], %2 offset:576\n\t" \
    "v_mfma_f32_16x16x32_bf16 v[136:139], v[68:71], v[100:103], v[136:139]\n\t" \
    "ds_read_b128 v[92:95], %1 offset:3136\n\t" \
    "s_waitcnt lgkmcnt(6)\n\t" \
    "v_mfma_f32_16x16x32_bf16 v[128:131], v[64:67], v[96:99], v[128:131]\n\t" \
    "ds_read_b128 v[84:87], %2 offset:1088\n\t" \
    "s_waitcnt lgkmcnt(6)\n\t" \
    "v_mfma_f32_16x16x32_bf16 v[140:143], v[72:75], v[100:103], v[140:143]\n\t" \
    "ds_read_b128 v[112:115], %2 offset:64\n\t" \
    "v_mfma_f32_16x16x32_bf16 v[132:135], v[68:71], v[96:99], v[132:135]\n\t" \
    "ds_read_b128 v[116:119], %1 offset:2624\n\t" \
    "v_mfma_f32_16x16x32_bf16 v[136:139], v[72:75], v[96:99], v[136:139]\n\t" \
    "ds_read_b128 v[120:123], %1 offset:2112\n\t" \
    "s_waitcnt lgkmcnt(8)\n\t" \
    "v_mfma_f32_16x16x32_bf16 v[140:143], v[76:79], v[96:99], v[140:143]\n\t" \
    "ds_read_b128 v[124:127], %2 offset:1600\n\t" \
    "s_waitcnt lgkmcnt(8)\n\t" \
    "v_mfma_f32_16x16x32_bf16 v[140:143], v[64:67], v[108:111], v[140:143]\n\t" \
    "v_mfma_f32_16x16x32_bf16 v[136:139], v[64:67], v[104:107], v[136:139]\n\t" \
    "s_waitcnt lgkmcnt(6)\n\t" \
    "v_mfma_f32_16x16x32_bf16 v[132:135], v[80:83], v[88:91], v[132:135]\n\t" \
    "ds_read_b128 v[64:67], %1 offset:3584\n\t" \
    "s_waitcnt lgkmcnt(5)\n\t" \
    "v_mfma_f32_16x16x32_bf16 v[140:143], v[92:95], v[84:87], v[140:143]\n\t" \
    "ds_read_b128 v[100:103], %2 offset:512\n\t" \
    "v_mfma_f32_16x16x32_bf16 v[136:139], v[92:95], v[88:91], v[136:139]\n\t" \
    "ds_read_b128 v[68:71], %1 offset:3072\n\t" \
    "s_waitcnt lgkmcnt(6)\n\t" \
    "v_mfma_f32_16x16x32_bf16 v[128:131], v[80:83], v[112:115], v[128:131]\n\t" \
    "ds_read_b128 v[104:107], %2 offset:1024\n\t" \
    "s_waitcnt lgkmcnt(6)\n\t" \
    "v_mfma_f32_16x16x32_bf16 v[140:143], v[116:119], v[88:91], v[140:143]\n\t" \
    "ds_read_b128 v[96:99], %2 offset:0\n\t" \
    "v_mfma_f32_16x16x32_bf16 v[132:135], v[92:95], v[112:115], v[132:135]\n\t" \
    "ds_read_b128 v[72:75], %1 offset:2560\n\t" \
    "v_mfma_f32_16x16x32_bf16 v[136:139], v[116:119], v[112:115], v[136:139]\n\t" \
    "ds_read_b128 v[76:79], %1 offset:2048\n\t" \
    "s_waitcnt lgkmcnt(8)\n\t" \
    "v_mfma_f32_16x16x32_bf16 v[140:143], v[120:123], v[112:115], v[140:143]\n\t" \
    "ds_read_b128 v[108:111], %2 offset:1536\n\t" \
    "s_waitcnt lgkmcnt(8)\n\t" \
    "v_mfma_f32_16x16x32_bf16 v[140:143], v[80:83], v[124:127], v[140:143]\n\t" \
    "v_mfma_f32_16x16x32_bf16 v[136:139], v[80:83], v[84:87], v[136:139]\n\t" \
    "s_waitcnt lgkmcnt(6)\n\t" \
    "v_mfma_f32_16x16x32_bf16 v[132:135], v[64:67], v[100:103], v[132:135]\n\t" \
    "ds_read_b128 v[88:91], %1 offset:3520\n\t" \
    "s_waitcnt lgkmcnt(5)\n\t" \
    "v_mfma_f32_16x16x32_bf16 v[140:143], v[68:71], v[104:107], v[140:143]\n\t" \
    "ds_read_b128 v[156:159], %2 offset:448\n\t" \
    "ds_read_b128 v[80:83], %1 offset:1984\n\t" \
    "v_mfma_f32_16x16x32_bf16 v[136:139], v[68:71], v[100:103], v[136:139]\n\t" \
    "ds_read_b128 v[120:123], %1 offset:3008\n\t" \
    "ds_read_b128 v[84:87], %1 offset:1472\n\t" \
    "s_waitcnt lgkmcnt(8)\n\t" \
    "v_mfma_f32_16x16x32_bf16 v[128:131], v[64:67], v[96:99], v[128:131]\n\t" \
    "ds_read_b128 v[124:127], %1 offset:4032\n\t" \
    "s_waitcnt lgkmcnt(0)\n\t" \
    "L_ub_%=:\n\t" \
    "ds_read_b32 v160, %4 offset:52\n\t" \
    "s_waitcnt lgkmcnt(0)\n\t" \
    "v_readfirstlane_b32 %0, v160\n\t" \
    "s_cmp_lt_u32 %0, 4\n\t" \
    "s_cbranch_scc1 L_ub_%=\n\t" \
    "ds_read_b128 v[92:95], %2 offset:2496\n\t" \
    "v_mfma_f32_16x16x32_bf16 v[140:143], v[72:75], v[100:103], v[140:143]\n\t" \
    "ds_read_b128 v[100:103], %2 offset:960\n\t" \
    "ds_read_b128 v[116:119], %2 offset:3008\n\t" \
    "v_mfma_f32_16x16x32_bf16 v[132:135], v[68:71], v[96:99], v[132:135]\n\t" \
    "ds_read_b128 v[152:155], %2 offset:1984\n\t" \
    "v_mfma_f32_16x16x32_bf16 v[136:139], v[72:75], v[96:99], v[136:139]\n\t" \
    "v_mfma_f32_16x16x32_bf16 v[140:143], v[76:79], v[96:99], v[140:143]\n\t" \
    "ds_read_b128 v[76:79], %1 offset:2496\n\t" \
    "v_mfma_f32_16x16x32_bf16 v[140:143], v[64:67], v[108:111], v[140:143]\n\t" \
    "v_mfma_f32_16x16x32_bf16 v[136:139], v[64:67], v[104:107], v[136:139]\n\t" \
    "ds_read_b128 v[104:107], %2 offset:1472\n\t" \
    "s_waitcnt lgkmcnt(5)\n\t" \
    "v_mfma_f32_16x16x32_bf16 v[148:151], v[88:91], v[92:95], 0\n\t" \
    "ds_read_b128 v[68:71], %1 offset:3456\n\t" \
    "v_mfma_f32_16x16x32_bf16 v[148:151], v[84:87], v[156:159], v[148:151]\n\t" \
    "ds_read_b128 v[84:87], %1 offset:1408\n\t" \
    "v_mfma_f32_16x16x32_bf16 v[144:147], v[80:83], v[156:159], 0\n\t" \
    "ds_read_b128 v[112:115], %2 offset:2432\n\t" \
    "s_waitcnt lgkmcnt(7)\n\t" \
    "v_mfma_f32_16x16x32_bf16 v[148:151], v[80:83], v[100:103], v[148:151]\n\t" \
    "ds_read_b128 v[80:83], %1 offset:1920\n\t" \
    "v_cvt_pk_bf16_f32 v160, v128, v129\n\t" \
    "v_cvt_pk_bf16_f32 v161, v130, v131\n\t" \
    "ds_write_b64 %3, v[160:161] offset:0\n\t" \
    "s_waitcnt lgkmcnt(6)\n\t" \
    "v_mfma_f32_16x16x32_bf16 v[144:147], v[76:79], v[100:103], v[144:147]\n\t" \
    "ds_read_b128 v[100:103], %2 offset:896\n\t" \
    "v_cvt_pk_bf16_f32 v162, v132, v133\n\t" \
    "v_cvt_pk_bf16_f32 v163, v134, v135\n\t" \
    "ds_write_b64 %3, v[162:163] offset:512\n\t" \
    "s_waitcnt lgkmcnt(7)\n\t" \
    "v_mfma_f32_16x16x32_bf16 v[148:151], v[76:79], v[104:107], v[148:151]\n\t" \
    "ds_read_b128 v[76:79], %1 offset:2432\n\t" \
    "v_cvt_pk_bf16_f32 v160, v136, v137\n\t" \
    "v_cvt_pk_bf16_f32 v161, v138, v139\n\t" \
    "ds_write_b64 %3, v[160:161] offset:1024\n\t" \
    "v_mfma_f32_16x16x32_bf16 v[144:147], v[120:123], v[104:107], v[144:147]\n\t" \
    "ds_read_b128 v[104:107], %2 offset:1408\n\t" \
    "v_cvt_pk_bf16_f32 v162, v140, v141\n\t" \
    "v_cvt_pk_bf16_f32 v163, v142, v143\n\t" \
    "ds_write_b64 %3, v[162:163] offset:1536\n\t" \
    "v_mfma_f32_16x16x32_bf16 v[148:151], v[124:127], v[116:119], v[148:151]\n\t" \
    "ds_read_b128 v[116:119], %2 offset:2944\n\t" \
    "v_mfma_f32_16x16x32_bf16 v[148:151], v[120:123], v[152:155], v[148:151]\n\t" \
    "ds_read_b128 v[96:99], %2 offset:384\n\t" \
    "v_mfma_f32_16x16x32_bf16 v[144:147], v[124:127], v[92:95], v[144:147]\n\t" \
    "ds_read_b128 v[72:75], %1 offset:2944\n\t" \
    "s_waitcnt lgkmcnt(3)\n\t" \
    "s_mov_b64 exec, 1\n\t" \
    "ds_add_u32 %4, %5 offset:0\n\t" \
    "s_mov_b64 exec, -1\n\t" \
    "v_mfma_f32_16x16x32_bf16 v[144:147], v[88:91], v[152:155], v[144:147]\n\t" \
    "ds_read_b128 v[64:67], %1 offset:3968\n\t" \
    "ds_read_b128 v[108:111], %2 offset:1920\n\t" \
    "v_mfma_f32_16x16x32_bf16 v[148:151], v[68:71], v[112:115], v[148:151]\n\t" \
    "ds_read_b128 v[88:91], %1 offset:3392\n\t" \
    "s_waitcnt lgkmcnt(5)\n\t" \
    "v_mfma_f32_16x16x32_bf16 v[148:151], v[84:87], v[96:99], v[148:151]\n\t" \
    "ds_read_b128 v[84:87], %1 offset:1344\n\t" \
    "v_mfma_f32_16x16x32_bf16 v[144:147], v[80:83], v[96:99], v[144:147]\n\t" \
    "ds_read_b128 v[92:95], %2 offset:2368\n\t" \
    "v_mfma_f32_16x16x32_bf16 v[148:151], v[80:83], v[100:103], v[148:151]\n\t" \
    "ds_read_b128 v[80:83], %1 offset:1856\n\t" \
    "v_mfma_f32_16x16x32_bf16 v[144:147], v[76:79], v[100:103], v[144:147]\n\t" \
    "ds_read_b128 v[100:103], %2 offset:832\n\t" \
    "v_mfma_f32_16x16x32_bf16 v[148:151], v[76:79], v[104:107], v[148:151]\n\t" \
    "ds_read_b128 v[76:79], %1 offset:2368\n\t" \
    "s_waitcnt lgkmcnt(9)\n\t" \
    "v_mfma_f32_16x16x32_bf16 v[144:147], v[72:75], v[104:107], v[144:147]\n\t" \
    "ds_read_b128 v[104:107], %2 offset:1344\n\t" \
    "s_waitcnt lgkmcnt(8)\n\t" \
    "v_mfma_f32_16x16x32_bf16 v[148:151], v[64:67], v[116:119], v[148:151]\n\t" \
    "ds_read_b128 v[116:119], %2 offset:2880\n\t" \
    "s_waitcnt lgkmcnt(8)\n\t" \
    "v_mfma_f32_16x16x32_bf16 v[148:151], v[72:75], v[108:111], v[148:151]\n\t" \
    "ds_read_b128 v[156:159], %2 offset:320\n\t" \
    "v_mfma_f32_16x16x32_bf16 v[144:147], v[64:67], v[112:115], v[144:147]\n\t" \
    "ds_read_b128 v[120:123], %1 offset:2880\n\t" \
    "v_mfma_f32_16x16x32_bf16 v[144:147], v[68:71], v[108:111], v[144:147]\n\t" \
    "ds_read_b128 v[124:127], %1 offset:3904\n\t" \
    "ds_read_b128 v[152:155], %2 offset:1856\n\t" \
    "s_waitcnt lgkmcnt(9)\n\t" \
    "v_mfma_f32_16x16x32_bf16 v[148:151], v[88:91], v[92:95], v[148:151]\n\t" \
    "ds_read_b128 v[68:71], %1 offset:3328\n\t" \
    "s_waitcnt lgkmcnt(4)\n\t" \
    "v_mfma_f32_16x16x32_bf16 v[148:151], v[84:87], v[156:159], v[148:151]\n\t" \
    "ds_read_b128 v[84:87], %1 offset:1280\n\t" \
    "v_mfma_f32_16x16x32_bf16 v[144:147], v[80:83], v[156:159], v[144:147]\n\t" \
    "ds_read_b128 v[112:115], %2 offset:2304\n\t" \
    "v_mfma_f32_16x16x32_bf16 v[148:151], v[80:83], v[100:103], v[148:151]\n\t" \
    "ds_read_b128 v[80:83], %1 offset:1792\n\t" \
    "v_mfma_f32_16x16x32_bf16 v[144:147], v[76:79], v[100:103], v[144:147]\n\t" \
    "ds_read_b128 v[100:103], %2 offset:768\n\t" \
    "v_mfma_f32_16x16x32_bf16 v[148:151], v[76:79], v[104:107], v[148:151]\n\t" \
    "ds_read_b128 v[76:79], %1 offset:2304\n\t" \
    "s_waitcnt lgkmcnt(8)\n\t" \
    "v_mfma_f32_16x16x32_bf16 v[144:147], v[120:123], v[104:107], v[144:147]\n\t" \
    "ds_read_b128 v[104:107], %2 offset:1280\n\t" \
    "s_waitcnt lgkmcnt(8)\n\t" \
    "v_mfma_f32_16x16x32_bf16 v[148:151], v[124:127], v[116:119], v[148:151]\n\t" \
    "ds_read_b128 v[116:119], %2 offset:2816\n\t" \
    "s_waitcnt lgkmcnt(8)\n\t" \
    "v_mfma_f32_16x16x32_bf16 v[148:151], v[120:123], v[152:155], v[148:151]\n\t" \
    "ds_read_b128 v[96:99], %2 offset:256\n\t" \
    "v_mfma_f32_16x16x32_bf16 v[144:147], v[124:127], v[92:95], v[144:147]\n\t" \
    "ds_read_b128 v[72:75], %1 offset:2816\n\t" \
    "v_mfma_f32_16x16x32_bf16 v[144:147], v[88:91], v[152:155], v[144:147]\n\t" \
    "ds_read_b128 v[64:67], %1 offset:3840\n\t" \
    "ds_read_b128 v[108:111], %2 offset:1792\n\t" \
    "s_waitcnt lgkmcnt(9)\n\t" \
    "v_mfma_f32_16x16x32_bf16 v[148:151], v[68:71], v[112:115], v[148:151]\n\t" \
    "ds_read_b128 v[88:91], %1 offset:3264\n\t" \
    "s_waitcnt lgkmcnt(4)\n\t" \
    "v_mfma_f32_16x16x32_bf16 v[148:151], v[84:87], v[96:99], v[148:151]\n\t" \
    "ds_read_b128 v[84:87], %1 offset:1216\n\t" \
    "v_mfma_f32_16x16x32_bf16 v[144:147], v[80:83], v[96:99], v[144:147]\n\t" \
    "ds_read_b128 v[92:95], %2 offset:2240\n\t" \
    "v_mfma_f32_16x16x32_bf16 v[148:151], v[80:83], v[100:103], v[148:151]\n\t" \
    "ds_read_b128 v[80:83], %1 offset:1728\n\t" \
    "v_mfma_f32_16x16x32_bf16 v[144:147], v[76:79], v[100:103], v[144:147]\n\t" \
    "ds_read_b128 v[100:103], %2 offset:704\n\t" \
    "v_mfma_f32_16x16x32_bf16 v[148:151], v[76:79], v[104:107], v[148:151]\n\t" \
    "ds_read_b128 v[76:79], %1 offset:2240\n\t" \
    "s_waitcnt lgkmcnt(8)\n\t" \
    "v_mfma_f32_16x16x32_bf16 v[144:147], v[72:75], v[104:107], v[144:147]\n\t" \
    "ds_read_b128 v[104:107], %2 offset:1216\n\t" \
    "s_waitcnt lgkmcnt(8)\n\t" \
    "v_mfma_f32_16x16x32_bf16 v[148:151], v[64:67], v[116:119], v[148:151]\n\t" \
    "ds_read_b128 v[116:119], %2 offset:2752\n\t" \
    "s_waitcnt lgkmcnt(8)\n\t" \
    "v_mfma_f32_16x16x32_bf16 v[148:151], v[72:75], v[108:111], v[148:151]\n\t" \
    "ds_read_b128 v[156:159], %2 offset:192\n\t" \
    "v_mfma_f32_16x16x32_bf16 v[144:147], v[64:67], v[112:115], v[144:147]\n\t" \
    "ds_read_b128 v[120:123], %1 offset:2752\n\t" \
    "v_mfma_f32_16x16x32_bf16 v[144:147], v[68:71], v[108:111], v[144:147]\n\t" \
    "ds_read_b128 v[124:127], %1 offset:3776\n\t" \
    "ds_read_b128 v[152:155], %2 offset:1728\n\t" \
    "s_waitcnt lgkmcnt(9)\n\t" \
    "v_mfma_f32_16x16x32_bf16 v[148:151], v[88:91], v[92:95], v[148:151]\n\t" \
    "ds_read_b128 v[68:71], %1 offset:3200\n\t" \
    "s_waitcnt lgkmcnt(4)\n\t" \
    "v_mfma_f32_16x16x32_bf16 v[148:151], v[84:87], v[156:159], v[148:151]\n\t" \
    "ds_read_b128 v[84:87], %1 offset:1152\n\t" \
    "v_mfma_f32_16x16x32_bf16 v[144:147], v[80:83], v[156:159], v[144:147]\n\t" \
    "ds_read_b128 v[112:115], %2 offset:2176\n\t" \
    "v_mfma_f32_16x16x32_bf16 v[148:151], v[80:83], v[100:103], v[148:151]\n\t" \
    "ds_read_b128 v[80:83], %1 offset:1664\n\t" \
    "v_mfma_f32_16x16x32_bf16 v[144:147], v[76:79], v[100:103], v[144:147]\n\t" \
    "ds_read_b128 v[100:103], %2 offset:640\n\t" \
    "v_mfma_f32_16x16x32_bf16 v[148:151], v[76:79], v[104:107], v[148:151]\n\t" \
    "ds_read_b128 v[76:79], %1 offset:2176\n\t" \
    "s_waitcnt lgkmcnt(8)\n\t" \
    "v_mfma_f32_16x16x32_bf16 v[144:147], v[120:123], v[104:107], v[144:147]\n\t" \
    "ds_read_b128 v[104:107], %2 offset:1152\n\t" \
    "s_waitcnt lgkmcnt(8)\n\t" \
    "v_mfma_f32_16x16x32_bf16 v[148:151], v[124:127], v[116:119], v[148:151]\n\t" \
    "ds_read_b128 v[116:119], %2 offset:2688\n\t" \
    "s_waitcnt lgkmcnt(8)\n\t" \
    "v_mfma_f32_16x16x32_bf16 v[148:151], v[120:123], v[152:155], v[148:151]\n\t" \
    "ds_read_b128 v[96:99], %2 offset:128\n\t" \
    "v_mfma_f32_16x16x32_bf16 v[144:147], v[124:127], v[92:95], v[144:147]\n\t" \
    "ds_read_b128 v[72:75], %1 offset:2688\n\t" \
    "v_mfma_f32_16x16x32_bf16 v[144:147], v[88:91], v[152:155], v[144:147]\n\t" \
    "ds_read_b128 v[64:67], %1 offset:3712\n\t" \
    "ds_read_b128 v[108:111], %2 offset:1664\n\t" \
    "s_waitcnt lgkmcnt(9)\n\t" \
    "v_mfma_f32_16x16x32_bf16 v[148:151], v[68:71], v[112:115], v[148:151]\n\t" \
    "ds_read_b128 v[88:91], %1 offset:3136\n\t" \
    "s_waitcnt lgkmcnt(4)\n\t" \
    "v_mfma_f32_16x16x32_bf16 v[148:151], v[84:87], v[96:99], v[148:151]\n\t" \
    "ds_read_b128 v[84:87], %1 offset:1088\n\t" \
    "v_mfma_f32_16x16x32_bf16 v[144:147], v[80:83], v[96:99], v[144:147]\n\t" \
    "ds_read_b128 v[92:95], %2 offset:2112\n\t" \
    "v_mfma_f32_16x16x32_bf16 v[148:151], v[80:83], v[100:103], v[148:151]\n\t" \
    "ds_read_b128 v[80:83], %1 offset:1600\n\t" \
    "v_mfma_f32_16x16x32_bf16 v[144:147], v[76:79], v[100:103], v[144:147]\n\t" \
    "ds_read_b128 v[100:103], %2 offset:576\n\t" \
    "v_mfma_f32_16x16x32_bf16 v[148:151], v[76:79], v[104:107], v[148:151]\n\t" \
    "ds_read_b128 v[76:79], %1 offset:2112\n\t" \
    "s_waitcnt lgkmcnt(8)\n\t" \
    "v_mfma_f32_16x16x32_bf16 v[144:147], v[72:75], v[104:107], v[144:147]\n\t" \
    "ds_read_b128 v[104:107], %2 offset:1088\n\t" \
    "s_waitcnt lgkmcnt(8)\n\t" \
    "v_mfma_f32_16x16x32_bf16 v[148:151], v[64:67], v[116:119], v[148:151]\n\t" \
    "ds_read_b128 v[116:119], %2 offset:2624\n\t" \
    "s_waitcnt lgkmcnt(8)\n\t" \
    "v_mfma_f32_16x16x32_bf16 v[148:151], v[72:75], v[108:111], v[148:151]\n\t" \
    "ds_read_b128 v[156:159], %2 offset:64\n\t" \
    "v_mfma_f32_16x16x32_bf16 v[144:147], v[64:67], v[112:115], v[144:147]\n\t" \
    "ds_read_b128 v[120:123], %1 offset:2624\n\t" \
    "v_mfma_f32_16x16x32_bf16 v[144:147], v[68:71], v[108:111], v[144:147]\n\t" \
    "ds_read_b128 v[124:127], %1 offset:3648\n\t" \
    "ds_read_b128 v[152:155], %2 offset:1600\n\t" \
    "s_waitcnt lgkmcnt(9)\n\t" \
    "v_mfma_f32_16x16x32_bf16 v[148:151], v[88:91], v[92:95], v[148:151]\n\t" \
    "ds_read_b128 v[68:71], %1 offset:3072\n\t" \
    "s_waitcnt lgkmcnt(4)\n\t" \
    "v_mfma_f32_16x16x32_bf16 v[148:151], v[84:87], v[156:159], v[148:151]\n\t" \
    "ds_read_b128 v[84:87], %1 offset:1024\n\t" \
    "v_mfma_f32_16x16x32_bf16 v[144:147], v[80:83], v[156:159], v[144:147]\n\t" \
    "ds_read_b128 v[112:115], %2 offset:2048\n\t" \
    "v_mfma_f32_16x16x32_bf16 v[148:151], v[80:83], v[100:103], v[148:151]\n\t" \
    "ds_read_b128 v[80:83], %1 offset:1536\n\t" \
    "v_mfma_f32_16x16x32_bf16 v[144:147], v[76:79], v[100:103], v[144:147]\n\t" \
    "ds_read_b128 v[100:103], %2 offset:512\n\t" \
    "v_mfma_f32_16x16x32_bf16 v[148:151], v[76:79], v[104:107], v[148:151]\n\t" \
    "ds_read_b128 v[76:79], %1 offset:2048\n\t" \
    "s_waitcnt lgkmcnt(8)\n\t" \
    "v_mfma_f32_16x16x32_bf16 v[144:147], v[120:123], v[104:107], v[144:147]\n\t" \
    "ds_read_b128 v[104:107], %2 offset:1024\n\t" \
    "s_waitcnt lgkmcnt(8)\n\t" \
    "v_mfma_f32_16x16x32_bf16 v[148:151], v[124:127], v[116:119], v[148:151]\n\t" \
    "ds_read_b128 v[116:119], %2 offset:2560\n\t" \
    "s_waitcnt lgkmcnt(8)\n\t" \
    "v_mfma_f32_16x16x32_bf16 v[148:151], v[120:123], v[152:155], v[148:151]\n\t" \
    "ds_read_b128 v[96:99], %2 offset:0\n\t" \
    "v_mfma_f32_16x16x32_bf16 v[144:147], v[124:127], v[92:95], v[144:147]\n\t" \
    "ds_read_b128 v[72:75], %1 offset:2560\n\t" \
    "v_mfma_f32_16x16x32_bf16 v[144:147], v[88:91], v[152:155], v[144:147]\n\t" \
    "ds_read_b128 v[64:67], %1 offset:3584\n\t" \
    "ds_read_b128 v[108:111], %2 offset:1536\n\t" \
    "s_waitcnt lgkmcnt(9)\n\t" \
    "v_mfma_f32_16x16x32_bf16 v[148:151], v[68:71], v[112:115], v[148:151]\n\t" \
    "ds_read_b128 v[88:91], %1 offset:960\n\t" \
    "s_waitcnt lgkmcnt(4)\n\t" \
    "v_mfma_f32_16x16x32_bf16 v[148:151], v[84:87], v[96:99], v[148:151]\n\t" \
    "ds_read_b128 v[84:87], %1 offset:1472\n\t" \
    "ds_read_b128 v[92:95], %1 offset:448\n\t" \
    "v_mfma_f32_16x16x32_bf16 v[144:147], v[80:83], v[96:99], v[144:147]\n\t" \
    "ds_read_b128 v[96:99], %2 offset:448\n\t" \
    "ds_read_b128 v[120:123], %2 offset:3520\n\t" \
    "v_mfma_f32_16x16x32_bf16 v[148:151], v[80:83], v[100:103], v[148:151]\n\t" \
    "ds_read_b128 v[80:83], %1 offset:1984\n\t" \
    "ds_read_b128 v[124:127], %2 offset:4032\n\t" \
    "v_mfma_f32_16x16x32_bf16 v[144:147], v[76:79], v[100:103], v[144:147]\n\t" \
    "ds_read_b128 v[100:103], %2 offset:960\n\t" \
    "v_mfma_f32_16x16x32_bf16 v[148:151], v[76:79], v[104:107], v[148:151]\n\t" \
    "ds_read_b128 v[76:79], %1 offset:2496\n\t" \
    "s_waitcnt lgkmcnt(11)\n\t" \
    "v_mfma_f32_16x16x32_bf16 v[144:147], v[72:75], v[104:107], v[144:147]\n\t" \
    "ds_read_b128 v[104:107], %2 offset:1472\n\t" \
    "s_waitcnt lgkmcnt(11)\n\t" \
    "v_mfma_f32_16x16x32_bf16 v[148:151], v[64:67], v[116:119], v[148:151]\n\t" \
    "ds_read_b128 v[116:119], %2 offset:3008\n\t" \
    "s_waitcnt lgkmcnt(11)\n\t" \
    "v_mfma_f32_16x16x32_bf16 v[148:151], v[72:75], v[108:111], v[148:151]\n\t" \
    "ds_read_b128 v[72:75], %1 offset:3008\n\t" \
    "v_mfma_f32_16x16x32_bf16 v[144:147], v[64:67], v[112:115], v[144:147]\n\t" \
    "ds_read_b128 v[64:67], %1 offset:4032\n\t" \
    "ds_read_b128 v[112:115], %2 offset:2496\n\t" \
    "v_mfma_f32_16x16x32_bf16 v[144:147], v[68:71], v[108:111], v[144:147]\n\t" \
    "ds_read_b128 v[68:71], %1 offset:3520\n\t" \
    "ds_read_b128 v[108:111], %2 offset:1984\n\t" \
    "s_waitcnt lgkmcnt(12)\n\t" \
    "v_mfma_f32_16x16x32_bf16 v[156:159], v[92:95], v[96:99], 0\n\t" \
    "ds_read_b128 v[92:95], %1 offset:384\n\t" \
    "v_mfma_f32_16x16x32_bf16 v[152:155], v[88:91], v[96:99], 0\n\t" \
    "ds_read_b128 v[96:99], %2 offset:384\n\t" \
    "s_waitcnt lgkmcnt(10)\n\t" \
    "v_mfma_f32_16x16x32_bf16 v[156:159], v[88:91], v[100:103], v[156:159]\n\t" \
    "ds_read_b128 v[88:91], %1 offset:896\n\t" \
    "v_mfma_f32_16x16x32_bf16 v[152:155], v[84:87], v[100:103], v[152:155]\n\t" \
    "ds_read_b128 v[100:103], %2 offset:896\n\t" \
    "v_cvt_pk_bf16_f32 v160, v144, v145\n\t" \
    "v_cvt_pk_bf16_f32 v161, v146, v147\n\t" \
    "ds_write_b64 %3, v[160:161] offset:2048\n\t" \
    "s_waitcnt lgkmcnt(11)\n\t" \
    "v_mfma_f32_16x16x32_bf16 v[156:159], v[84:87], v[104:107], v[156:159]\n\t" \
    "ds_read_b128 v[84:87], %1 offset:1408\n\t" \
    "v_cvt_pk_bf16_f32 v162, v148, v149\n\t" \
    "v_cvt_pk_bf16_f32 v163, v150, v151\n\t" \
    "ds_write_b64 %3, v[162:163] offset:2560\n\t" \
    "s_waitcnt lgkmcnt(10)\n\t" \
    "v_mfma_f32_16x16x32_bf16 v[156:159], v[64:67], v[124:127], v[156:159]\n\t" \
    "ds_read_b128 v[124:127], %2 offset:3968\n\t" \
    "v_mfma_f32_16x16x32_bf16 v[152:155], v[80:83], v[104:107], v[152:155]\n\t" \
    "ds_read_b128 v[104:107], %2 offset:1408\n\t" \
    "v_mfma_f32_16x16x32_bf16 v[152:155], v[64:67], v[120:123], v[152:155]\n\t" \
    "ds_read_b128 v[64:67], %1 offset:3968\n\t" \
    "s_waitcnt lgkmcnt(3)\n\t" \
    "s_mov_b64 exec, 1\n\t" \
    "ds_add_u32 %4, %5 offset:4\n\t" \
    "s_mov_b64 exec, -1\n\t" \
    "v_mfma_f32_16x16x32_bf16 v[156:159], v[80:83], v[108:111], v[156:159]\n\t" \
    "ds_read_b128 v[80:83], %1 offset:1920\n\t" \
    "v_mfma_f32_16x16x32_bf16 v[156:159], v[68:71], v[120:123], v[156:159]\n\t" \
    "ds_read_b128 v[120:123], %2 offset:3456\n\t" \
    "v_mfma_f32_16x16x32_bf16 v[152:155], v[76:79], v[108:111], v[152:155]\n\t" \
    "ds_read_b128 v[108:111], %2 offset:1920\n\t" \
    "v_mfma_f32_16x16x32_bf16 v[152:155], v[68:71], v[116:119], v[152:155]\n\t" \
    "ds_read_b128 v[68:71], %1 offset:3456\n\t" \
    "v_mfma_f32_16x16x32_bf16 v[156:159], v[76:79], v[112:115], v[156:159]\n\t" \
    "ds_read_b128 v[76:79], %1 offset:2432\n\t" \
    "v_mfma_f32_16x16x32_bf16 v[156:159], v[72:75], v[116:119], v[156:159]\n\t" \
    "ds_read_b128 v[116:119], %2 offset:2944\n\t" \
    "v_mfma_f32_16x16x32_bf16 v[152:155], v[72:75], v[112:115], v[152:155]\n\t" \
    "ds_read_b128 v[72:75], %1 offset:2944\n\t" \
    "ds_read_b128 v[112:115], %2 offset:2432\n\t" \
    "v_mfma_f32_16x16x32_bf16 v[156:159], v[92:95], v[96:99], v[156:159]\n\t" \
    "ds_read_b128 v[92:95], %1 offset:320\n\t" \
    "v_mfma_f32_16x16x32_bf16 v[152:155], v[88:91], v[96:99], v[152:155]\n\t" \
    "ds_read_b128 v[96:99], %2 offset:320\n\t" \
    "v_mfma_f32_16x16x32_bf16 v[156:159], v[88:91], v[100:103], v[156:159]\n\t" \
    "ds_read_b128 v[88:91], %1 offset:832\n\t" \
    "v_mfma_f32_16x16x32_bf16 v[152:155], v[84:87], v[100:103], v[152:155]\n\t" \
    "ds_read_b128 v[100:103], %2 offset:832\n\t" \
    "s_waitcnt lgkmcnt(14)\n\t" \
    "v_mfma_f32_16x16x32_bf16 v[156:159], v[84:87], v[104:107], v[156:159]\n\t" \
    "ds_read_b128 v[84:87], %1 offset:1344\n\t" \
    "s_waitcnt lgkmcnt(14)\n\t" \
    "v_mfma_f32_16x16x32_bf16 v[156:159], v[64:67], v[124:127], v[156:159]\n\t" \
    "ds_read_b128 v[124:127], %2 offset:3904\n\t" \
    "s_waitcnt lgkmcnt(13)\n\t" \
    "v_mfma_f32_16x16x32_bf16 v[152:155], v[80:83], v[104:107], v[152:155]\n\t" \
    "ds_read_b128 v[104:107], %2 offset:1344\n\t" \
    "s_waitcnt lgkmcnt(13)\n\t" \
    "v_mfma_f32_16x16x32_bf16 v[152:155], v[64:67], v[120:123], v[152:155]\n\t" \
    "ds_read_b128 v[64:67], %1 offset:3904\n\t" \
    "s_waitcnt lgkmcnt(13)\n\t" \
    "v_mfma_f32_16x16x32_bf16 v[156:159], v[80:83], v[108:111], v[156:159]\n\t" \
    "ds_read_b128 v[80:83], %1 offset:1856\n\t" \
    "s_waitcnt lgkmcnt(13)\n\t" \
    "v_mfma_f32_16x16x32_bf16 v[156:159], v[68:71], v[120:123], v[156:159]\n\t" \
    "ds_read_b128 v[120:123], %2 offset:3392\n\t" \
    "s_waitcnt lgkmcnt(13)\n\t" \
    "v_mfma_f32_16x16x32_bf16 v[152:155], v[76:79], v[108:111], v[152:155]\n\t" \
    "ds_read_b128 v[108:111], %2 offset:1856\n\t" \
    "s_waitcnt lgkmcnt(13)\n\t" \
    "v_mfma_f32_16x16x32_bf16 v[152:155], v[68:71], v[116:119], v[152:155]\n\t" \
    "ds_read_b128 v[68:71], %1 offset:3392\n\t" \
    "s_waitcnt lgkmcnt(12)\n\t" \
    "v_mfma_f32_16x16x32_bf16 v[156:159], v[76:79], v[112:115], v[156:159]\n\t" \
    "ds_read_b128 v[76:79], %1 offset:2368\n\t" \
    "v_mfma_f32_16x16x32_bf16 v[156:159], v[72:75], v[116:119], v[156:159]\n\t" \
    "ds_read_b128 v[116:119], %2 offset:2880\n\t" \
    "v_mfma_f32_16x16x32_bf16 v[152:155], v[72:75], v[112:115], v[152:155]\n\t" \
    "ds_read_b128 v[72:75], %1 offset:2880\n\t" \
    "ds_read_b128 v[112:115], %2 offset:2368\n\t" \
    "s_waitcnt lgkmcnt(14)\n\t" \
    "v_mfma_f32_16x16x32_bf16 v[156:159], v[92:95], v[96:99], v[156:159]\n\t" \
    "ds_read_b128 v[92:95], %1 offset:256\n\t" \
    "s_waitcnt lgkmcnt(14)\n\t" \
    "v_mfma_f32_16x16x32_bf16 v[152:155], v[88:91], v[96:99], v[152:155]\n\t" \
    "ds_read_b128 v[96:99], %2 offset:256\n\t" \
    "s_waitcnt lgkmcnt(14)\n\t" \
    "v_mfma_f32_16x16x32_bf16 v[156:159], v[88:91], v[100:103], v[156:159]\n\t" \
    "ds_read_b128 v[88:91], %1 offset:768\n\t" \
    "s_waitcnt lgkmcnt(14)\n\t" \
    "v_mfma_f32_16x16x32_bf16 v[152:155], v[84:87], v[100:103], v[152:155]\n\t" \
    "ds_read_b128 v[100:103], %2 offset:768\n\t" \
    "s_waitcnt lgkmcnt(13)\n\t" \
    "v_mfma_f32_16x16x32_bf16 v[156:159], v[84:87], v[104:107], v[156:159]\n\t" \
    "ds_read_b128 v[84:87], %1 offset:1280\n\t" \
    "s_waitcnt lgkmcnt(13)\n\t" \
    "v_mfma_f32_16x16x32_bf16 v[156:159], v[64:67], v[124:127], v[156:159]\n\t" \
    "ds_read_b128 v[124:127], %2 offset:3840\n\t" \
    "s_waitcnt lgkmcnt(13)\n\t" \
    "v_mfma_f32_16x16x32_bf16 v[152:155], v[80:83], v[104:107], v[152:155]\n\t" \
    "ds_read_b128 v[104:107], %2 offset:1280\n\t" \
    "s_waitcnt lgkmcnt(13)\n\t" \
    "v_mfma_f32_16x16x32_bf16 v[152:155], v[64:67], v[120:123], v[152:155]\n\t" \
    "ds_read_b128 v[64:67], %1 offset:3840\n\t" \
    "s_waitcnt lgkmcnt(13)\n\t" \
    "v_mfma_f32_16x16x32_bf16 v[156:159], v[80:83], v[108:111], v[156:159]\n\t" \
    "ds_read_b128 v[80:83], %1 offset:1792\n\t" \
    "s_waitcnt lgkmcnt(13)\n\t" \
    "v_mfma_f32_16x16x32_bf16 v[156:159], v[68:71], v[120:123], v[156:159]\n\t" \
    "ds_read_b128 v[120:123], %2 offset:3328\n\t" \
    "s_waitcnt lgkmcnt(13)\n\t" \
    "v_mfma_f32_16x16x32_bf16 v[152:155], v[76:79], v[108:111], v[152:155]\n\t" \
    "ds_read_b128 v[108:111], %2 offset:1792\n\t" \
    "s_waitcnt lgkmcnt(13)\n\t" \
    "v_mfma_f32_16x16x32_bf16 v[152:155], v[68:71], v[116:119], v[152:155]\n\t" \
    "ds_read_b128 v[68:71], %1 offset:3328\n\t" \
    "s_waitcnt lgkmcnt(12)\n\t" \
    "v_mfma_f32_16x16x32_bf16 v[156:159], v[76:79], v[112:115], v[156:159]\n\t" \
    "ds_read_b128 v[76:79], %1 offset:2304\n\t" \
    "v_mfma_f32_16x16x32_bf16 v[156:159], v[72:75], v[116:119], v[156:159]\n\t" \
    "ds_read_b128 v[116:119], %2 offset:2816\n\t" \
    "v_mfma_f32_16x16x32_bf16 v[152:155], v[72:75], v[112:115], v[152:155]\n\t" \
    "ds_read_b128 v[72:75], %1 offset:2816\n\t" \
    "ds_read_b128 v[112:115], %2 offset:2304\n\t" \
    "s_waitcnt lgkmcnt(14)\n\t" \
    "v_mfma_f32_16x16x32_bf16 v[156:159], v[92:95], v[96:99], v[156:159]\n\t" \
    "ds_read_b128 v[92:95], %1 offset:192\n\t" \
    "s_waitcnt lgkmcnt(14)\n\t" \
    "v_mfma_f32_16x16x32_bf16 v[152:155], v[88:91], v[96:99], v[152:155]\n\t" \
    "ds_read_b128 v[96:99], %2 offset:192\n\t" \
    "s_waitcnt lgkmcnt(14)\n\t" \
    "v_mfma_f32_16x16x32_bf16 v[156:159], v[88:91], v[100:103], v[156:159]\n\t" \
    "ds_read_b128 v[88:91], %1 offset:704\n\t" \
    "s_waitcnt lgkmcnt(14)\n\t" \
    "v_mfma_f32_16x16x32_bf16 v[152:155], v[84:87], v[100:103], v[152:155]\n\t" \
    "ds_read_b128 v[100:103], %2 offset:704\n\t" \
    "s_waitcnt lgkmcnt(13)\n\t" \
    "v_mfma_f32_16x16x32_bf16 v[156:159], v[84:87], v[104:107], v[156:159]\n\t" \
    "ds_read_b128 v[84:87], %1 offset:1216\n\t" \
    "s_waitcnt lgkmcnt(13)\n\t" \
    "v_mfma_f32_16x16x32_bf16 v[156:159], v[64:67], v[124:127], v[156:159]\n\t" \
    "ds_read_b128 v[124:127], %2 offset:3776\n\t" \
    "s_waitcnt lgkmcnt(13)\n\t" \
    "v_mfma_f32_16x16x32_bf16 v[152:155], v[80:83], v[104:107], v[152:155]\n\t" \
    "ds_read_b128 v[104:107], %2 offset:1216\n\t" \
    "s_waitcnt lgkmcnt(13)\n\t" \
    "v_mfma_f32_16x16x32_bf16 v[152:155], v[64:67], v[120:123], v[152:155]\n\t" \
    "ds_read_b128 v[64:67], %1 offset:3776\n\t" \
    "s_waitcnt lgkmcnt(13)\n\t" \
    "v_mfma_f32_16x16x32_bf16 v[156:159], v[80:83], v[108:111], v[156:159]\n\t" \
    "ds_read_b128 v[80:83], %1 offset:1728\n\t" \
    "s_waitcnt lgkmcnt(13)\n\t" \
    "v_mfma_f32_16x16x32_bf16 v[156:159], v[68:71], v[120:123], v[156:159]\n\t" \
    "ds_read_b128 v[120:123], %2 offset:3264\n\t" \
    "s_waitcnt lgkmcnt(13)\n\t" \
    "v_mfma_f32_16x16x32_bf16 v[152:155], v[76:79], v[108:111], v[152:155]\n\t" \
    "ds_read_b128 v[108:111], %2 offset:1728\n\t" \
    "s_waitcnt lgkmcnt(13)\n\t" \
    "v_mfma_f32_16x16x32_bf16 v[152:155], v[68:71], v[116:119], v[152:155]\n\t" \
    "ds_read_b128 v[68:71], %1 offset:3264\n\t" \
    "s_waitcnt lgkmcnt(12)\n\t" \
    "v_mfma_f32_16x16x32_bf16 v[156:159], v[76:79], v[112:115], v[156:159]\n\t" \
    "ds_read_b128 v[76:79], %1 offset:2240\n\t" \
    "v_mfma_f32_16x16x32_bf16 v[156:159], v[72:75], v[116:119], v[156:159]\n\t" \
    "ds_read_b128 v[116:119], %2 offset:2752\n\t" \
    "v_mfma_f32_16x16x32_bf16 v[152:155], v[72:75], v[112:115], v[152:155]\n\t" \
    "ds_read_b128 v[72:75], %1 offset:2752\n\t" \
    "ds_read_b128 v[112:115], %2 offset:2240\n\t" \
    "s_waitcnt lgkmcnt(14)\n\t" \
    "v_mfma_f32_16x16x32_bf16 v[156:159], v[92:95], v[96:99], v[156:159]\n\t" \
    "ds_read_b128 v[92:95], %1 offset:128\n\t" \
    "s_waitcnt lgkmcnt(14)\n\t" \
    "v_mfma_f32_16x16x32_bf16 v[152:155], v[88:91], v[96:99], v[152:155]\n\t" \
    "ds_read_b128 v[96:99], %2 offset:128\n\t" \
    "s_waitcnt lgkmcnt(14)\n\t" \
    "v_mfma_f32_16x16x32_bf16 v[156:159], v[88:91], v[100:103], v[156:159]\n\t" \
    "ds_read_b128 v[88:91], %1 offset:640\n\t" \
    "s_waitcnt lgkmcnt(14)\n\t" \
    "v_mfma_f32_16x16x32_bf16 v[152:155], v[84:87], v[100:103], v[152:155]\n\t" \
    "ds_read_b128 v[100:103], %2 offset:640\n\t" \
    "s_waitcnt lgkmcnt(13)\n\t" \
    "v_mfma_f32_16x16x32_bf16 v[156:159], v[84:87], v[104:107], v[156:159]\n\t" \
    "ds_read_b128 v[84:87], %1 offset:1152\n\t" \
    "s_waitcnt lgkmcnt(13)\n\t" \
    "v_mfma_f32_16x16x32_bf16 v[156:159], v[64:67], v[124:127], v[156:159]\n\t" \
    "ds_read_b128 v[124:127], %2 offset:3712\n\t" \
    "s_waitcnt lgkmcnt(13)\n\t" \
    "v_mfma_f32_16x16x32_bf16 v[152:155], v[80:83], v[104:107], v[152:155]\n\t" \
    "ds_read_b128 v[104:107], %2 offset:1152\n\t" \
    "s_waitcnt lgkmcnt(13)\n\t" \
    "v_mfma_f32_16x16x32_bf16 v[152:155], v[64:67], v[120:123], v[152:155]\n\t" \
    "ds_read_b128 v[64:67], %1 offset:3712\n\t" \
    "s_waitcnt lgkmcnt(13)\n\t" \
    "v_mfma_f32_16x16x32_bf16 v[156:159], v[80:83], v[108:111], v[156:159]\n\t" \
    "ds_read_b128 v[80:83], %1 offset:1664\n\t" \
    "s_waitcnt lgkmcnt(13)\n\t" \
    "v_mfma_f32_16x16x32_bf16 v[156:159], v[68:71], v[120:123], v[156:159]\n\t" \
    "ds_read_b128 v[120:123], %2 offset:3200\n\t" \
    "s_waitcnt lgkmcnt(13)\n\t" \
    "v_mfma_f32_16x16x32_bf16 v[152:155], v[76:79], v[108:111], v[152:155]\n\t" \
    "ds_read_b128 v[108:111], %2 offset:1664\n\t" \
    "s_waitcnt lgkmcnt(13)\n\t" \
    "v_mfma_f32_16x16x32_bf16 v[152:155], v[68:71], v[116:119], v[152:155]\n\t" \
    "ds_read_b128 v[68:71], %1 offset:3200\n\t" \
    "s_waitcnt lgkmcnt(12)\n\t" \
    "v_mfma_f32_16x16x32_bf16 v[156:159], v[76:79], v[112:115], v[156:159]\n\t" \
    "ds_read_b128 v[76:79], %1 offset:2176\n\t" \
    "v_mfma_f32_16x16x32_bf16 v[156:159], v[72:75], v[116:119], v[156:159]\n\t" \
    "ds_read_b128 v[116:119], %2 offset:2688\n\t" \
    "v_mfma_f32_16x16x32_bf16 v[152:155], v[72:75], v[112:115], v[152:155]\n\t" \
    "ds_read_b128 v[72:75], %1 offset:2688\n\t" \
    "ds_read_b128 v[112:115], %2 offset:2176\n\t" \
    "s_waitcnt lgkmcnt(14)\n\t" \
    "v_mfma_f32_16x16x32_bf16 v[156:159], v[92:95], v[96:99], v[156:159]\n\t" \
    "ds_read_b128 v[92:95], %1 offset:64\n\t" \
    "s_waitcnt lgkmcnt(14)\n\t" \
    "v_mfma_f32_16x16x32_bf16 v[152:155], v[88:91], v[96:99], v[152:155]\n\t" \
    "ds_read_b128 v[96:99], %2 offset:64\n\t" \
    "s_waitcnt lgkmcnt(14)\n\t" \
    "v_mfma_f32_16x16x32_bf16 v[156:159], v[88:91], v[100:103], v[156:159]\n\t" \
    "ds_read_b128 v[88:91], %1 offset:576\n\t" \
    "s_waitcnt lgkmcnt(14)\n\t" \
    "v_mfma_f32_16x16x32_bf16 v[152:155], v[84:87], v[100:103], v[152:155]\n\t" \
    "ds_read_b128 v[100:103], %2 offset:576\n\t" \
    "s_waitcnt lgkmcnt(13)\n\t" \
    "v_mfma_f32_16x16x32_bf16 v[156:159], v[84:87], v[104:107], v[156:159]\n\t" \
    "ds_read_b128 v[84:87], %1 offset:1088\n\t" \
    "s_waitcnt lgkmcnt(13)\n\t" \
    "v_mfma_f32_16x16x32_bf16 v[156:159], v[64:67], v[124:127], v[156:159]\n\t" \
    "ds_read_b128 v[124:127], %2 offset:3648\n\t" \
    "s_waitcnt lgkmcnt(13)\n\t" \
    "v_mfma_f32_16x16x32_bf16 v[152:155], v[80:83], v[104:107], v[152:155]\n\t" \
    "ds_read_b128 v[104:107], %2 offset:1088\n\t" \
    "s_waitcnt lgkmcnt(13)\n\t" \
    "v_mfma_f32_16x16x32_bf16 v[152:155], v[64:67], v[120:123], v[152:155]\n\t" \
    "ds_read_b128 v[64:67], %1 offset:3648\n\t" \
    "s_waitcnt lgkmcnt(13)\n\t" \
    "v_mfma_f32_16x16x32_bf16 v[156:159], v[80:83], v[108:111], v[156:159]\n\t" \
    "ds_read_b128 v[80:83], %1 offset:1600\n\t" \
    "s_waitcnt lgkmcnt(13)\n\t" \
    "v_mfma_f32_16x16x32_bf16 v[156:159], v[68:71], v[120:123], v[156:159]\n\t" \
    "ds_read_b128 v[120:123], %2 offset:3136\n\t" \
    "s_waitcnt lgkmcnt(13)\n\t" \
    "v_mfma_f32_16x16x32_bf16 v[152:155], v[76:79], v[108:111], v[152:155]\n\t" \
    "ds_read_b128 v[108:111], %2 offset:1600\n\t" \
    "s_waitcnt lgkmcnt(13)\n\t" \
    "v_mfma_f32_16x16x32_bf16 v[152:155], v[68:71], v[116:119], v[152:155]\n\t" \
    "ds_read_b128 v[68:71], %1 offset:3136\n\t" \
    "s_waitcnt lgkmcnt(12)\n\t" \
    "v_mfma_f32_16x16x32_bf16 v[156:159], v[76:79], v[112:115], v[156:159]\n\t" \
    "ds_read_b128 v[76:79], %1 offset:2112\n\t" \
    "v_mfma_f32_16x16x32_bf16 v[156:159], v[72:75], v[116:119], v[156:159]\n\t" \
    "ds_read_b128 v[116:119], %2 offset:2624\n\t" \
    "v_mfma_f32_16x16x32_bf16 v[152:155], v[72:75], v[112:115], v[152:155]\n\t" \
    "ds_read_b128 v[72:75], %1 offset:2624\n\t" \
    "ds_read_b128 v[112:115], %2 offset:2112\n\t" \
    "s_waitcnt lgkmcnt(14)\n\t" \
    "v_mfma_f32_16x16x32_bf16 v[156:159], v[92:95], v[96:99], v[156:159]\n\t" \
    "ds_read_b128 v[92:95], %1 offset:0\n\t" \
    "s_waitcnt lgkmcnt(14)\n\t" \
    "v_mfma_f32_16x16x32_bf16 v[152:155], v[88:91], v[96:99], v[152:155]\n\t" \
    "ds_read_b128 v[96:99], %2 offset:0\n\t" \
    "s_waitcnt lgkmcnt(14)\n\t" \
    "v_mfma_f32_16x16x32_bf16 v[156:159], v[88:91], v[100:103], v[156:159]\n\t" \
    "ds_read_b128 v[88:91], %1 offset:512\n\t" \
    "s_waitcnt lgkmcnt(14)\n\t" \
    "v_mfma_f32_16x16x32_bf16 v[152:155], v[84:87], v[100:103], v[152:155]\n\t" \
    "ds_read_b128 v[100:103], %2 offset:512\n\t" \
    "s_waitcnt lgkmcnt(13)\n\t" \
    "v_mfma_f32_16x16x32_bf16 v[156:159], v[84:87], v[104:107], v[156:159]\n\t" \
    "ds_read_b128 v[84:87], %1 offset:1024\n\t" \
    "s_waitcnt lgkmcnt(13)\n\t" \
    "v_mfma_f32_16x16x32_bf16 v[156:159], v[64:67], v[124:127], v[156:159]\n\t" \
    "ds_read_b128 v[124:127], %2 offset:3584\n\t" \
    "s_waitcnt lgkmcnt(13)\n\t" \
    "v_mfma_f32_16x16x32_bf16 v[152:155], v[80:83], v[104:107], v[152:155]\n\t" \
    "ds_read_b128 v[104:107], %2 offset:1024\n\t" \
    "s_waitcnt lgkmcnt(13)\n\t" \
    "v_mfma_f32_16x16x32_bf16 v[152:155], v[64:67], v[120:123], v[152:155]\n\t" \
    "ds_read_b128 v[64:67], %1 offset:3584\n\t" \
    "s_waitcnt lgkmcnt(13)\n\t" \
    "v_mfma_f32_16x16x32_bf16 v[156:159], v[80:83], v[108:111], v[156:159]\n\t" \
    "ds_read_b128 v[80:83], %1 offset:1536\n\t" \
    "s_waitcnt lgkmcnt(13)\n\t" \
    "v_mfma_f32_16x16x32_bf16 v[156:159], v[68:71], v[120:123], v[156:159]\n\t" \
    "ds_read_b128 v[120:123], %2 offset:3072\n\t" \
    "s_waitcnt lgkmcnt(13)\n\t" \
    "v_mfma_f32_16x16x32_bf16 v[152:155], v[76:79], v[108:111], v[152:155]\n\t" \
    "ds_read_b128 v[108:111], %2 offset:1536\n\t" \
    "s_waitcnt lgkmcnt(13)\n\t" \
    "v_mfma_f32_16x16x32_bf16 v[152:155], v[68:71], v[116:119], v[152:155]\n\t" \
    "ds_read_b128 v[68:71], %1 offset:3072\n\t" \
    "s_waitcnt lgkmcnt(12)\n\t" \
    "v_mfma_f32_16x16x32_bf16 v[156:159], v[76:79], v[112:115], v[156:159]\n\t" \
    "ds_read_b128 v[76:79], %1 offset:2048\n\t" \
    "v_mfma_f32_16x16x32_bf16 v[156:159], v[72:75], v[116:119], v[156:159]\n\t" \
    "ds_read_b128 v[116:119], %2 offset:2560\n\t" \
    "v_mfma_f32_16x16x32_bf16 v[152:155], v[72:75], v[112:115], v[152:155]\n\t" \
    "ds_read_b128 v[72:75], %1 offset:2560\n\t" \
    "ds_read_b128 v[112:115], %2 offset:2048\n\t" \
    "s_waitcnt lgkmcnt(14)\n\t" \
    "v_mfma_f32_16x16x32_bf16 v[156:159], v[92:95], v[96:99], v[156:159]\n\t" \
    "s_waitcnt lgkmcnt(13)\n\t" \
    "v_mfma_f32_16x16x32_bf16 v[152:155], v[88:91], v[96:99], v[152:155]\n\t" \
    "s_waitcnt lgkmcnt(12)\n\t" \
    "v_mfma_f32_16x16x32_bf16 v[156:159], v[88:91], v[100:103], v[156:159]\n\t" \
    "s_waitcnt lgkmcnt(11)\n\t" \
    "v_mfma_f32_16x16x32_bf16 v[152:155], v[84:87], v[100:103], v[152:155]\n\t" \
    "s_waitcnt lgkmcnt(9)\n\t" \
    "v_mfma_f32_16x16x32_bf16 v[156:159], v[84:87], v[104:107], v[156:159]\n\t" \
    "s_waitcnt lgkmcnt(8)\n\t" \
    "v_mfma_f32_16x16x32_bf16 v[156:159], v[64:67], v[124:127], v[156:159]\n\t" \
    "s_waitcnt lgkmcnt(7)\n\t" \
    "v_mfma_f32_16x16x32_bf16 v[152:155], v[80:83], v[104:107], v[152:155]\n\t" \
    "s_waitcnt lgkmcnt(6)\n\t" \
    "v_mfma_f32_16x16x32_bf16 v[152:155], v[64:67], v[120:123], v[152:155]\n\t" \
    "s_waitcnt lgkmcnt(5)\n\t" \
    "v_mfma_f32_16x16x32_bf16 v[156:159], v[80:83], v[108:111], v[156:159]\n\t" \
    "s_waitcnt lgkmcnt(4)\n\t" \
    "v_mfma_f32_16x16x32_bf16 v[156:159], v[68:71], v[120:123], v[156:159]\n\t" \
    "s_waitcnt lgkmcnt(3)\n\t" \
    "v_mfma_f32_16x16x32_bf16 v[152:155], v[76:79], v[108:111], v[152:155]\n\t" \
    "s_waitcnt lgkmcnt(2)\n\t" \
    "v_mfma_f32_16x16x32_bf16 v[152:155], v[68:71], v[116:119], v[152:155]\n\t" \
    "s_waitcnt lgkmcnt(0)\n\t" \
    "v_mfma_f32_16x16x32_bf16 v[156:159], v[76:79], v[112:115], v[156:159]\n\t" \
    "v_mfma_f32_16x16x32_bf16 v[156:159], v[72:75], v[116:119], v[156:159]\n\t" \
    "v_mfma_f32_16x16x32_bf16 v[152:155], v[72:75], v[112:115], v[152:155]\n\t" \
    "s_nop 7\n\t" \
    "v_cvt_pk_bf16_f32 v160, v152, v153\n\t" \
    "v_cvt_pk_bf16_f32 v161, v154, v155\n\t" \
    "ds_write_b64 %3, v[160:161] offset:3072\n\t" \
    "v_cvt_pk_bf16_f32 v162, v156, v157\n\t" \
    "v_cvt_pk_bf16_f32 v163, v158, v159\n\t" \
    "ds_write_b64 %3, v[162:163] offset:3584\n\t" \
    "s_waitcnt lgkmcnt(0)\n\t" \
    "s_mov_b64 exec, 1\n\t" \
    "ds_add_u32 %4, %5 offset:8\n\t" \
    "s_mov_b64 exec, -1\n\t" \
    "s_waitcnt lgkmcnt(0)\n\t" \
    ""
#define CONV_CLOBBERS "v64", "v65", "v66", "v67", "v68", "v69", "v70", "v71", "v72", "v73", "v74", "v75", "v76", "v77", "v78", "v79", "v80", "v81", "v82", "v83", "v84", "v85", "v86", "v87", "v88", "v89", "v90", "v91", "v92", "v93", "v94", "v95", "v96", "v97", "v98", "v99", "v100", "v101", "v102", "v103", "v104", "v105", "v106", "v107", "v108", "v109", "v110", "v111", "v112", "v113", "v114", "v115", "v116", "v117", "v118", "v119", "v120", "v121", "v122", "v123", "v124", "v125", "v126", "v127", "v128", "v129", "v130", "v131", "v132", "v133", "v134", "v135", "v136", "v137", "v138", "v139", "v140", "v141", "v142", "v143", "v144", "v145", "v146", "v147", "v148", "v149", "v150", "v151", "v152", "v153", "v154", "v155", "v156", "v157", "v158", "v159", "v160", "v161", "v162", "v163"
#define PASS_OF_TILE(t) ((t) <= 3 ? 0 : ((t) <= 5 ? 1 : 2))
#define LAST_PASS_FIRST_TILE 6

#define NTHREADS 768
#define CODE_BYTES 17280
#define G_OFF TILE_OFF

__device__ __forceinline__ void epi_quad(lptr sm, int T0, int b, u32* __restrict__ out32, int p, float dA0, float dA1, float dS0, float dS1) {
    const lptr yp = sm + Y_OFF + T0 * 2 + b * 8;
    const v2u ya0 = *(LAS v2u*)(yp), yb0 = *(LAS v2u*)(yp + 4096), ya1 = *(LAS v2u*)(yp + 8192), yb1 = *(LAS v2u*)(yp + 12288);
    const lptr ub = sm + U_OFF + b * U_COPY + 512 + T0 * 2;
    const v4u kv0 = *(LAS v4u*)(ub), x0 = *(LAS v4u*)(ub + U_UNIT), kv1 = *(LAS v4u*)(ub + 2 * U_UNIT), x1 = *(LAS v4u*)(ub + 3 * U_UNIT);
    const v4u gg = *(LAS v4u*)(sm + G_OFF + T0 * 4 + b * 16);
    u32* o = out32 + (u32)((T0 + b) * 256 + p);
#pragma unroll
    for (int e = 0; e < 4; e++) {
        const float fya0 = (e & 1) ? bfhi(ya0[e >> 1]) : bflo(ya0[e >> 1]), fyb0 = (e & 1) ? bfhi(yb0[e >> 1]) : bflo(yb0[e >> 1]);
        const float fya1 = (e & 1) ? bfhi(ya1[e >> 1]) : bflo(ya1[e >> 1]), fyb1 = (e & 1) ? bfhi(yb1[e >> 1]) : bflo(yb1[e >> 1]);
        const float fkv0 = bfhi(kv0[e]), fx0 = bfhi(x0[e]), fkv1 = bfhi(kv1[e]), fx1 = bfhi(x1[e]);
        const float g = bflo(gg[e]), g1 = bfhi(gg[e]);
        const float a0 = fx0 * (fya0 + fkv0 * dA0), b0 = fyb0 + fx0 * dS0;
        const float a1 = fx1 * (fya1 + fkv1 * dA1), b1 = fyb1 + fx1 * dS1;
        o[e * 512] = pk(g * a0 + g1 * b0, g * a1 + g1 * b1);
    }
}

__device__ __forceinline__ void fetch_u(lptr sm, lptr cnt, char* ws, __amdgpu_buffer_rsrc_t ut_rsrc, int p, int tb, int lane, u64 epoch
) {
    const int pidx = (tb >> 6) - 1 + min(lane, 4);
    GAS u64* inbox = (GAS u64*)(ws + WS_INBOX) + p * 32 + max(pidx, 0);
    u64 va = __hip_atomic_load(inbox, __ATOMIC_RELAXED, __HIP_MEMORY_SCOPE_AGENT);
#pragma unroll 1
    for (u32 spins = 0; spins < SPIN_CAP; spins++) {
        const u64 vb = __hip_atomic_load(inbox, __ATOMIC_RELAXED, __HIP_MEMORY_SCOPE_AGENT);
        if (__all(va == epoch)) break;
        va = __hip_atomic_load(inbox, __ATOMIC_RELAXED, __HIP_MEMORY_SCOPE_AGENT);
        if (__all(vb == epoch)) break;
    }
    asm volatile("" ::: "memory");
    v4u ud[2];
    u32 up[2];
    const int t = tb + (lane & 31) * 8;
#pragma unroll
    for (int i = 0; i < 2; i++) {
        const int u = i * 2 + (lane >> 5);
        const u32 off = (u32)((((u & 1) * D_ + p * 2 + (u >> 1)) * S_ + t) * 2);
        ud[i] = __builtin_amdgcn_raw_buffer_load_b128(ut_rsrc, off, 0, 16);
        up[i] = __builtin_amdgcn_raw_buffer_load_b32(ut_rsrc, t ? off - 4u : off, 0, 16);
    }
    wait_lds(cnt + C_T, 8);
#pragma unroll
    for (int i = 0; i < 2; i++) {
        const int u = i * 2 + (lane >> 5);
        const lptr dst = sm + U_OFF + u * U_UNIT + 512 + t * 2;
        const u32 prev = t ? up[i] : 0u;
        *(LAS v4u*)(dst + U_COPY) = ud[i];
        *(LAS v4u*)(dst) = (v4u){__builtin_amdgcn_alignbit(ud[i][0], prev, 16), __builtin_amdgcn_alignbit(ud[i][1], ud[i][0], 16),
                                 __builtin_amdgcn_alignbit(ud[i][2], ud[i][1], 16), __builtin_amdgcn_alignbit(ud[i][3], ud[i][2], 16)};
    }
}

__device__ __forceinline__ void epi_row(lptr sm, int t, u32* __restrict__ out32, int p, float dA0, float dA1, float dS0, float dS1) {
    const int T0 = t & ~7, e = t & 7;
    const lptr yp = sm + Y_OFF + T0 * 2 + (e & 1) * 8 + (e >> 2) * 4 + ((e >> 1) & 1) * 2;
    const float ya0 = bf1(*(LAS u16*)(yp)), yb0 = bf1(*(LAS u16*)(yp + 4096)), ya1 = bf1(*(LAS u16*)(yp + 8192)), yb1 = bf1(*(LAS u16*)(yp + 12288));
    const lptr ub = sm + U_OFF + U_COPY + 512 + t * 2;
    const float kv0 = bf1(*(LAS u16*)(ub)), x0 = bf1(*(LAS u16*)(ub + U_UNIT)), kv1 = bf1(*(LAS u16*)(ub + 2 * U_UNIT)), x1 = bf1(*(LAS u16*)(ub + 3 * U_UNIT));
    const u32 gg = *(LAS u32*)(sm + G_OFF + (t >> 3) * 32 + (t & 1) * 16 + ((t >> 1) & 3) * 4);
    const float g = bflo(gg), g1 = bfhi(gg);
    const float a0 = x0 * (ya0 + kv0 * dA0), b0 = yb0 + x0 * dS0;
    const float a1 = x1 * (ya1 + kv1 * dA1), b1 = yb1 + x1 * dS1;
    out32[(u32)(t * 256 + p)] = pk(g * a0 + g1 * b0, g * a1 + g1 * b1);
}

__global__ void __launch_bounds__(NTHREADS, 3)
fused_kernel(const u16* __restrict__ X, const u16* __restrict__ Kin, const u16* __restrict__ V, const u16* __restrict__ shiftk,
             const u16* __restrict__ hA, const u16* __restrict__ ssmD, const u16* __restrict__ Are, const u16* __restrict__ Aim,
             const u16* __restrict__ Bre, const u16* __restrict__ Bim, const u16* __restrict__ Cre, const u16* __restrict__ Cim,
             const u16* __restrict__ logdt, const u16* __restrict__ Dskip, const u16* __restrict__ gatew,
             char* __restrict__ ws, u32* __restrict__ out32) {
    __shared__ __attribute__((aligned(16))) char smem_[LDS_BYTES];
    const lptr sm = (lptr)smem_;
    const int tid = threadIdx.x;
    const int bid = blockIdx.x;
    const int lane = tid & 63;
    const int w = __builtin_amdgcn_readfirstlane(tid >> 6);
    const int dg = bid & 7, tg = bid >> 3;
    const int p = dg * 32 + tg;
    const lptr cnt = sm + F_OFF;
    u32 pf = 0;
    if (w >= 4 && w < 4 + (CODE_BYTES + 8191) / 8192) {
        u64 pc;
        asm volatile("s_getpc_b64 %0" : "=s"(pc));
        const u32 off = min((u32)(tid - 256) * 128u, (u32)CODE_BYTES - 4u);
        asm volatile("global_load_dword %0, %1, %2" : "=v"(pf) : "v"(off), "s"(pc & ~127ull) : "memory");
    }
    if (tid < 16) *(LAS u32*)(cnt + tid * 4) = 0;
    asm volatile("" :: "s"(X), "s"(Kin), "s"(V), "s"(shiftk), "s"(hA), "s"(ssmD), "s"(Are), "s"(Aim), "s"(Bre), "s"(Bim), "s"(Cre), "s"(Cim),
                 "s"(logdt), "s"(Dskip), "s"(gatew), "s"(ws), "s"(out32));

    if (w < 4) {
        __builtin_amdgcn_s_setprio(2);
        const int cp = tid & 31, rb = tid >> 5;
        const int c = dg * 64 + cp * 2;
        u32 kk[11], vv[8], xx[8];
        {
            const __amdgpu_buffer_rsrc_t k_rsrc = __builtin_amdgcn_make_buffer_rsrc((void*)Kin, 0, S_ * D_ * 2, 0x00020000);
            const u32 o = (u32)((tg * 64 + rb * 8) * D_ + c) * 2u;
            kk[0] = __builtin_amdgcn_raw_buffer_load_b32(k_rsrc, o - 3072u, 0, 0);
            kk[1] = __builtin_amdgcn_raw_buffer_load_b32(k_rsrc, o - 2048u, 0, 0);
            kk[2] = __builtin_amdgcn_raw_buffer_load_b32(k_rsrc, o - 1024u, 0, 0);
#pragma unroll
            for (int j = 0; j < 8; j++) kk[3 + j] = ldg<u32>(Kin, o + j * 1024);
#pragma unroll
            for (int j = 0; j < 8; j++) vv[j] = ldg<u32>(V, o + j * 1024);
#pragma unroll
            for (int j = 0; j < 8; j++) xx[j] = ldg<u32>(X, o + j * 1024);
        }
        const v4u sk = ldg<v4u>(shiftk, (u32)c * 8u);
        __builtin_amdgcn_sched_barrier(0);
        RAW_BARRIER();
        const int tgate = bid * 8 + w * 2;
        v4u gx0, gx1, gw;
        u64 E;
        {
            float s0[4] = {bflo(sk[0]), bfhi(sk[0]), bflo(sk[1]), bfhi(sk[1])};
            float s1[4] = {bflo(sk[2]), bfhi(sk[2]), bflo(sk[3]), bfhi(sk[3])};
            u32 kvp[8];
#pragma unroll
            for (int j = 0; j < 8; j++) {
                float a0 = bflo(kk[j]) * s0[3] + bflo(kk[j + 1]) * s0[2] + bflo(kk[j + 2]) * s0[1] + bflo(kk[j + 3]) * s0[0];
                float a1 = bfhi(kk[j]) * s1[3] + bfhi(kk[j + 1]) * s1[2] + bfhi(kk[j + 2]) * s1[1] + bfhi(kk[j + 3]) * s1[0];
                kvp[j] = pk(a0 * bflo(vv[j]), a1 * bfhi(vv[j]));
            }
            __builtin_amdgcn_sched_barrier(0);
            gx0 = ldg<v4u>(X, (u32)(tgate * D_ + lane * 8) * 2u); gx1 = ldg<v4u>(X, (u32)((tgate + 1) * D_ + lane * 8) * 2u);
            gw = ldg<v4u>(gatew, (u32)lane * 16u);
            E = __hip_atomic_load((GAS u64*)(ws + WS_E), __ATOMIC_RELAXED, __HIP_MEMORY_SCOPE_AGENT);
            __builtin_amdgcn_sched_barrier(0);
            const lptr base = sm + TILE_OFF + (cp * 2) * 128 + ((rb ^ (cp & 7)) * 16);
            v4u a, b;
#pragma unroll
            for (int j = 0; j < 4; j++) {
                a[j] = __builtin_amdgcn_perm(kvp[2 * j + 1], kvp[2 * j], 0x05040100);
                b[j] = __builtin_amdgcn_perm(kvp[2 * j + 1], kvp[2 * j], 0x07060302);
            }
            *(LAS v4u*)(base) = a; *(LAS v4u*)(base + 128) = b;
#pragma unroll
            for (int j = 0; j < 4; j++) {
                a[j] = __builtin_amdgcn_perm(xx[2 * j + 1], xx[2 * j], 0x05040100);
                b[j] = __builtin_amdgcn_perm(xx[2 * j + 1], xx[2 * j], 0x07060302);
            }
            *(LAS v4u*)(base + 8192) = a; *(LAS v4u*)(base + 8192 + 128) = b;
        }
        lds_arrive(cnt + C_TILE);
        wait_lds(cnt + C_TILE, 4);
        const __amdgpu_buffer_rsrc_t ut_rsrc = __builtin_amdgcn_make_buffer_rsrc(ws + WS_UT, 0, 2 * D_ * S_ * 2, 0x00020000);
        {
            v4u d[4];
#pragma unroll
            for (int i = 0; i < 4; i++) {
                const int ch = (i & 1) * 32 + (tid >> 3), ck = tid & 7;
                d[i] = *(LAS v4u*)(sm + TILE_OFF + (i >> 1) * 8192 + ch * 128 + ((ck ^ ((ch >> 1) & 7)) * 16));
            }
#pragma unroll
            for (int i = 0; i < 4; i++) {
                const int ch = (i & 1) * 32 + (tid >> 3), ck = tid & 7;
                __builtin_amdgcn_raw_buffer_store_b128(d[i], ut_rsrc, (u32)((((i >> 1) * D_ + dg * 64 + ch) * S_ + tg * 64 + ck * 8) * 2), 0, 16);
            }
        }
        lds_arrive(cnt + C_TR);
        const u64 epoch = uniform64(E) + 1ull;
        if (w == 0) {
            if (lane == 0) *(LAS u64*)(sm + EP_OFF) = epoch;
            lds_arrive(cnt + C_EP);
        }
        u32 gb;
        {
            float s0 = 0.f, s1 = 0.f;
#pragma unroll
            for (int j = 0; j < 4; j++) {
                u32 p0 = pk(bflo(gx0[j]) * bflo(gw[j]), bfhi(gx0[j]) * bfhi(gw[j]));
                u32 p1 = pk(bflo(gx1[j]) * bflo(gw[j]), bfhi(gx1[j]) * bfhi(gw[j]));
                s0 += bflo(p0) + bfhi(p0);
                s1 += bflo(p1) + bfhi(p1);
            }
            s0 = dpp_add<0x111>(s0); s1 = dpp_add<0x111>(s1);
            s0 = dpp_add<0x112>(s0); s1 = dpp_add<0x112>(s1);
            s0 = dpp_add<0x114>(s0); s1 = dpp_add<0x114>(s1);
            s0 = dpp_add<0x118>(s0); s1 = dpp_add<0x118>(s1);
            const int i0 = __float_as_int(s0), i1 = __float_as_int(s1);
            const float t0_ = __int_as_float(__builtin_amdgcn_readlane(i0, 15)) + __int_as_float(__builtin_amdgcn_readlane(i0, 31)) +
                              __int_as_float(__builtin_amdgcn_readlane(i0, 47)) + __int_as_float(__builtin_amdgcn_readlane(i0, 63));
            const float t1_ = __int_as_float(__builtin_amdgcn_readlane(i1, 15)) + __int_as_float(__builtin_amdgcn_readlane(i1, 31)) +
                              __int_as_float(__builtin_amdgcn_readlane(i1, 47)) + __int_as_float(__builtin_amdgcn_readlane(i1, 63));
            const float z = rbf((lane & 1) ? t1_ : t0_);
            const float g = 1.f / (1.f + __expf(-z));
            gb = pk(g, g) & 0xffff;
        }
        asm volatile("s_waitcnt vmcnt(0)" ::: "memory");
        if (lane < 8) {
            const int cons = dg * 32 + (lane >> 2) * 16 + w * 4 + (lane & 3);
            __hip_atomic_store((GAS u64*)(ws + WS_INBOX) + cons * 32 + tg, epoch, __ATOMIC_RELAXED, __HIP_MEMORY_SCOPE_AGENT);
        }
        if (lane < 2) __hip_atomic_store((GAS u64*)(ws + WS_GATE) + tgate + lane, (epoch << 16) | gb, __ATOMIC_RELAXED, __HIP_MEMORY_SCOPE_AGENT);
        wait_lds(cnt + C_T, 8);
        *(LAS v4u*)(sm + U_OFF + (tid >> 5) * U_COPY + (tid & 31) * 16) = (v4u){0, 0, 0, 0};
        fetch_u(sm, cnt, ws, ut_rsrc, p, w * 256, lane, epoch
        );
        lds_arrive(cnt + C_UA);
        wait_lds(cnt + C_UA, 4);
        wait_lds(cnt + C_H, 8);
        __builtin_amdgcn_s_setprio(0);
        const int g4 = lane >> 4, n = lane & 15;
        const lptr ha = sm + w * H_UNIT + (n & 3) * H_COPY + 48 - 16 * (n >> 2) + 16 * g4;
        const lptr ub = sm + U_OFF + w * U_UNIT + (n & 1) * U_COPY + 512 + 64 * (n >> 1) + 16 * g4 - 7 * 64;
        const lptr yb = sm + Y_OFF + w * 4096 + 64 * (n >> 1) + 16 * g4 + 8 * (n & 1);
        u32 stmp;
        asm volatile(CONV_ASM : "=&s"(stmp) : "v"((u32)(size_t)ha), "v"((u32)(size_t)ub), "v"((u32)(size_t)yb), "v"((u32)(size_t)cnt), "v"(1u) : CONV_CLOBBERS, "memory");
    } else {
        const int kt = tid - 256;
        const int half = __builtin_amdgcn_readfirstlane(kt >> 8);
        const int kch = (kt >> 7) & 1, kn = kt & 127;
        const int kd = p * 2 + kch;
        const u32 pi = (u32)(kd * N_ + kn) * 2u;
        const u16 p_ldt = ldg<u16>(logdt, (u32)kd * 2u);
        const u16 p_are = ldg<u16>(Are, pi), p_aim = ldg<u16>(Aim, pi), p_bre = ldg<u16>(Bre, pi), p_bim = ldg<u16>(Bim, pi), p_cre = ldg<u16>(Cre, pi), p_cim = ldg<u16>(Cim, pi);
        const int hck = kt & 255;
        const u32 ho = (u32)((p * 2 + half) * S_ + hck * 8) * 2u;
        v4u hd;
        v3u hm;
        const u32 dAp = *(const u32*)(ssmD + p * 2), dSp = *(const u32*)(Dskip + p * 2);
        __builtin_amdgcn_sched_barrier(0);
        RAW_BARRIER();
        u32 lv[14];
#pragma unroll
        for (int j = 0; j < 14; j++) {
            const int l = hck * 8 - 6 + j;
            lv[j] = (u32)rbf((float)(j < 6 ? max(l, 0) : l)) * 2;
        }
        {
            const float dt = rbf(expf(bf1(p_ldt)));
            const float a = dt * bf1(p_are), b = dt * bf1(p_aim);
            const float br = dt * bf1(p_bre), bi = dt * bf1(p_bim);
            const float cre = bf1(p_cre), cim = bf1(p_cim);
            v2f CB;
            CB[0] = cre * br - cim * bi;
            CB[1] = -(cre * bi + cim * br);
            const float a2 = a * 1.4426950408889634f;
            const float rh = b * 0.15915494309189535f;
            const float rl = __builtin_fmaf(b, 0.15915494309189535f, -rh) + b * 6.4206383e-9f;
            const lptr tab = sm + R2_OFF + kch * TAB_CH + kn * 4;
            asm volatile("" :: "v"(a2), "v"(rh), "v"(rl), "v"(CB), "v"(pf));
            __builtin_amdgcn_sched_barrier(0);
            hd = ldg<v4u>(hA, ho);
            hm = ldg<v3u>(hA, ho - (hck ? 12u : 0u));
            __builtin_amdgcn_sched_barrier(0);
#define PUT(row, v) (*(LAS u32*)(tab + (row) * TAB_PITCH) = pk2(v))
#define PUTC(row, v) (*(LAS u32*)(tab + (row) * TAB_PITCH) = pk((v)[0], -(v)[1]))
            v2f t0_, t1_;
            if (half == 0) {
                v2f E0 = epow<true>(a2, rh, rl, 1.f), E3 = epow<true>(a2, rh, rl, 8.f), E7 = epow<true>(a2, rh, rl, 128.f);
                v2f E1, E2, F[8], F8;
                csqr2(E1, E0, F[2], E3);
                csqr2(E2, E1, F[4], F[2]);
                v2f P[8];
                P[0] = CB; F[1] = E3;
                cmul2(P[1], P[0], E0, P[2], P[0], E1);
                cmul2(P[3], P[1], E1, P[4], P[0], E2);
                cmul2(P[5], P[1], E2, P[6], P[2], E2);
                cmul2(P[7], P[3], E2, F8, F[4], F[4]);
#pragma unroll
                for (int e = 0; e < 8; e += 2) {
                    PUT(e, P[e]); PUT(e + 1, P[e + 1]);
                    cmul2(t0_, P[e], E7, t1_, P[e + 1], E7);
                    PUT(8 + e, t0_); PUT(9 + e, t1_);
                }
                F[0][0] = 1.f; F[0][1] = 0.f;
                cmul2(F[3], F[1], F[2], F[5], F[4], F[1]);
                cmul2(F[6], F[4], F[2], t0_, F8, F[1]);
                PUTC(48 + 9, t0_);
                cmul2(F[7], F[4], F[3], t0_, F8, F[2]);
                PUTC(48 + 10, t0_);
#pragma unroll
                for (int e = 0; e < 8; e++) PUTC(48 + e, F[e]);
                PUTC(48 + 8, F8);
                cmul2(t0_, F8, F[3], t1_, F8, F[4]); PUTC(48 + 11, t0_); PUTC(48 + 12, t1_);
                cmul2(t0_, F8, F[5], t1_, F8, F[6]); PUTC(48 + 13, t0_); PUTC(48 + 14, t1_);
                cmul2(t0_, F8, F[7], t1_, F8, F[7]); PUTC(48 + 15, t0_);
            } else {
                v2f E1 = epow<true>(a2, rh, rl, 2.f), E7 = epow<true>(a2, rh, rl, 128.f), E9 = epow<true>(a2, rh, rl, 512.f);
                v2f E2, E8, E10, E11, P2, P4, P6, W3, R[4], Q0, Q1, T0, T1, T2, T3;
                csqr2(E2, E1, E8, E7);
                cmul2(E10, E9, E9, P2, CB, E1);
                cmul2(E11, E10, E10, P4, CB, E2);
                cmul2(P6, P2, E2, W3, E7, E8);
                cmul2(R[0], CB, E8, R[1], P2, E8);
                cmul2(R[2], P4, E8, R[3], P6, E8);
#pragma unroll
                for (int e = 0; e < 4; e += 2) {
                    PUT(16 + e, R[e]); PUT(17 + e, R[e + 1]);
                    cmul2(t0_, R[e], E7, t1_, R[e + 1], E7);
                    PUT(20 + e, t0_); PUT(21 + e, t1_);
                }
                cmul2(Q0, CB, E9, Q1, P4, E9);
                PUT(24, Q0); PUT(28, Q1);
                cmul2(t0_, Q0, E7, t1_, Q1, E7); PUT(25, t0_); PUT(29, t1_);
                cmul2(t0_, Q0, E8, t1_, Q1, E8); PUT(26, t0_); PUT(30, t1_);
                cmul2(t0_, Q0, W3, t1_, Q1, W3); PUT(27, t0_); PUT(31, t1_);
                cmul2(T0, CB, E10, t0_, CB, E11); PUT(32, T0); PUT(40, t0_);
                cmul2(T1, T0, E7, T2, T0, E8); PUT(33, T1); PUT(34, T2);
                cmul2(T3, T0, W3, t0_, T0, E9); PUT(35, T3); PUT(36, t0_);
                cmul2(t0_, T1, E9, t1_, T2, E9); PUT(37, t0_); PUT(38, t1_);
                cmul2(t0_, T3, E9, t1_, T3, E9); PUT(39, t0_);
            }
        }
        lds_arrive(cnt + C_TAB);
        if (kt < 48) {
            const int uc = kt / 3, ck = kt - uc * 3;
            *(LAS v4u*)(sm + uc * H_COPY + (256 + ck) * 16) = (v4u){0, 0, 0, 0};
        }
        const int id = w - 4;
        if (id < 6) {
            const int c2 = (id >= 3) ? 1 : 0, mt = id - c2 * 3;
            const int g4 = lane >> 4;
            int base, step;
            if (mt == 0) { base = ((g4 >> 1) << 7) + ((g4 & 1) << 2); step = 1; }
            else if (mt == 1) { base = (g4 < 2) ? (256 + (g4 << 7)) : (512 + ((g4 & 1) << 2)); step = (g4 < 2) ? 2 : 128; }
            else { base = (g4 < 2) ? (1024 + (g4 << 9)) : (2048 + ((g4 & 1) << 3)); step = 128; }
            const lptr T = sm + T_OFF + c2 * T_CH + (base + 8 * (lane & 15)) * 2;
            const lptr ga = sm + R2_OFF + c2 * TAB_CH + (mt * 16 + (lane & 15)) * TAB_PITCH + (lane >> 4) * 16;
            const lptr fb = sm + R2_OFF + c2 * TAB_CH + (48 + (lane & 15)) * TAB_PITCH + (lane >> 4) * 16;
            wait_lds(cnt + C_TAB, 8);
            v8bf A[8], B[8];
#pragma unroll
            for (int s = 0; s < 8; s++) { A[s] = *(LAS v8bf*)(ga + s * 64); B[s] = *(LAS v8bf*)(fb + s * 64); }
            v4f acc0 = {0.f, 0.f, 0.f, 0.f}, acc1 = {0.f, 0.f, 0.f, 0.f};
#pragma unroll
            for (int s = 0; s < 8; s += 2) {
                acc0 = __builtin_amdgcn_mfma_f32_16x16x32_bf16(A[s], B[s], acc0, 0, 0, 0);
                acc1 = __builtin_amdgcn_mfma_f32_16x16x32_bf16(A[s + 1], B[s + 1], acc1, 0, 0, 0);
            }
            const u32 o01 = pk(acc0[0] + acc1[0], acc0[1] + acc1[1]), o23 = pk(acc0[2] + acc1[2], acc0[3] + acc1[3]);
            *(LAS u16*)(T) = (u16)o01;
            *(LAS u16*)(T + step * 2) = (u16)(o01 >> 16);
            *(LAS u16*)(T + step * 4) = (u16)o23;
            *(LAS u16*)(T + step * 6) = (u16)(o23 >> 16);
        }
        lds_arrive(cnt + C_T);
        wait_lds(cnt + C_T, 8);
        {
            const lptr T = sm + T_OFF + half * T_CH;
            u32 hv[14];
#pragma unroll
            for (int j = 0; j < 14; j++) {
                const u32 x = *(LAS u16*)(T + lv[j]);
                hv[j] = (j < 6 && hck == 0) ? 0u : x;
            }
            u32 r[7];
#pragma unroll
            for (int i = 0; i < 7; i++) r[i] = hv[13 - 2 * i] | (hv[12 - 2 * i] << 16);
            const lptr dst = sm + (half * 2 + 1) * H_UNIT + (255 - hck) * 16;
            *(LAS v4u*)(dst) = (v4u){r[3], r[4], r[5], r[6]};
            *(LAS v4u*)(dst + H_COPY) = (v4u){r[2], r[3], r[4], r[5]};
            *(LAS v4u*)(dst + 2 * H_COPY) = (v4u){r[1], r[2], r[3], r[4]};
            *(LAS v4u*)(dst + 3 * H_COPY) = (v4u){r[0], r[1], r[2], r[3]};
        }
        {
            if (hck == 0) { hm[0] = 0; hm[1] = 0; hm[2] = 0; }
            u32 r[7];
            r[0] = rot16(hd[3]); r[1] = rot16(hd[2]); r[2] = rot16(hd[1]); r[3] = rot16(hd[0]);
            r[4] = rot16(hm[2]); r[5] = rot16(hm[1]); r[6] = rot16(hm[0]);
            const lptr dst = sm + (half * 2) * H_UNIT + (255 - hck) * 16;
            *(LAS v4u*)(dst) = (v4u){r[3], r[4], r[5], r[6]};
            *(LAS v4u*)(dst + H_COPY) = (v4u){r[2], r[3], r[4], r[5]};
            *(LAS v4u*)(dst + 2 * H_COPY) = (v4u){r[1], r[2], r[3], r[4]};
            *(LAS v4u*)(dst + 3 * H_COPY) = (v4u){r[0], r[1], r[2], r[3]};
        }
        lds_arrive(cnt + C_H);
        wait_lds(cnt + C_EP, 1);
        const u64 epl = *(LAS u64*)(sm + EP_OFF);
        const u64 epoch = uniform64(epl);
        const float dA0 = bflo(dAp), dA1 = bfhi(dAp), dS0 = bflo(dSp), dS1 = bfhi(dSp);
        if (w >= 8) {
            const __amdgpu_buffer_rsrc_t ut_rsrc = __builtin_amdgcn_make_buffer_rsrc(ws + WS_UT, 0, 2 * D_ * S_ * 2, 0x00020000);
            fetch_u(sm, cnt, ws, ut_rsrc, p, 1024 + (w - 8) * 256, lane, epoch
            );
            lds_arrive(cnt + C_UB);
            __builtin_amdgcn_s_setprio(3);
        } else {
            __builtin_amdgcn_s_setprio(3);
            GAS u64* gr = (GAS u64*)(ws + WS_GATE) + kt;
            const u64 tag = epoch & 0xFFFFFFFFFFFFull;
            u32 gv[8];
#pragma unroll 1
            for (u32 spins = 0; spins < SPIN_CAP; spins++) {
                bool ok = true;
#pragma unroll
                for (int i = 0; i < 8; i++) {
                    const u64 v = __hip_atomic_load(gr + i * 256, __ATOMIC_RELAXED, __HIP_MEMORY_SCOPE_AGENT);
                    gv[i] = (u32)v & 0xffff;
                    ok &= (v >> 16) == tag;
                }
                if (__all(ok)) break;
                __builtin_amdgcn_s_sleep(1);
            }
            wait_lds(cnt + C_TR, 4);
#pragma unroll
            for (int i = 0; i < 8; i++) {
                const int t = kt + i * 256;
                *(LAS u32*)(sm + G_OFF + (t >> 3) * 32 + (t & 1) * 16 + ((t >> 1) & 3) * 4) = gv[i] | (pk(0.f, 1.f - bf1((u16)gv[i])) & 0xffff0000u);
            }
            lds_arrive(cnt + C_G);
        }
        wait_lds(cnt + C_G, 4);
        if (bid == 0 && tid == 256) __hip_atomic_store((GAS u64*)(ws + WS_E), epoch, __ATOMIC_RELAXED, __HIP_MEMORY_SCOPE_AGENT);
        if (w - 4 < LAST_PASS_FIRST_TILE) {
            wait_lds(cnt + 4 * PASS_OF_TILE(w - 4), 4);
            epi_quad(sm, (kt >> 1) * 8, kt & 1, out32, p, dA0, dA1, dS0, dS1);
        }
        wait_lds(cnt + 4 * PASS_OF_TILE(7), 4);
        if (kt < (8 - LAST_PASS_FIRST_TILE) * 256) epi_row(sm, LAST_PASS_FIRST_TILE * 256 + kt, out32, p, dA0, dA1, dS0, dS1);
    }
}

extern "C" void kernel_launch(void* const* d_in, const int* in_sizes, int n_in,
                              void* d_out, int out_size, void* d_ws, size_t ws_size,
                              hipStream_t stream) {
    fused_kernel<<<256, NTHREADS, 0, stream>>>((const u16*)d_in[0], (const u16*)d_in[1], (const u16*)d_in[2], (const u16*)d_in[3],
                                          (const u16*)d_in[4], (const u16*)d_in[5], (const u16*)d_in[7], (const u16*)d_in[8],
                                          (const u16*)d_in[9], (const u16*)d_in[10], (const u16*)d_in[11], (const u16*)d_in[12],
                                          (const u16*)d_in[13], (const u16*)d_in[14], (const u16*)d_in[15], (char*)d_ws, (u32*)d_out);
}
```
